```python
import torch

def _softmax(x, axis=-1):
    return torch.softmax(x, dim=axis)
def _gelu(x):
    return torch.nn.functional.gelu(x, approximate="tanh")
def _silu(x):
    return torch.nn.functional.silu(x)
def _sigmoid(x):
    return torch.sigmoid(x)
def _layernorm(x, axis=-1, eps=1e-5):
    m = x.mean(dim=axis, keepdim=True)
    v = x.var(dim=axis, keepdim=True, unbiased=False)
    return (x - m) / torch.sqrt(v + eps)
def _scatter_add(tgt, idx, src, axis=0):
    return tgt.clone().index_add_(axis, idx, src)
def _segment_sum(src, seg, n):
    out = torch.zeros((n,) + src.shape[1:], dtype=src.dtype)
    return out.index_add_(0, seg, src)
def _pad(x, pw):
    flat = tuple(v for pair in reversed(pw) for v in pair)
    return torch.nn.functional.pad(x, flat)

kernel_name = 'sequential_preLN__mha_block_cached__dense_activation_squared_relu__decode'

def setup_inputs():

    _w = (lambda a: (lambda t: t.to(torch.bfloat16) if t.is_floating_point() else t)(torch.as_tensor(a)))
    def _A_setup():
        B, Sq, D = 2, 1, 640
        H, Sk, Dh = 32, 8192, 192
        return {
            "x": _w((lambda *s: torch.randn(*s, dtype=torch.bfloat16) * 0.05)(B, Sq, D)),
            "k_cache": _w((lambda *s: torch.randn(*s, dtype=torch.bfloat16) * 3.2)(B, H, Sk, Dh)),
            "v_cache": _w((lambda *s: torch.randn(*s, dtype=torch.bfloat16) * 0.4)(B, H, Sk, Dh)),
            "wq": _w((lambda *s: torch.randn(*s, dtype=torch.bfloat16) * 3.2)(D, H * Dh) * (D ** -0.5)),
            "wo": _w((lambda *s: torch.randn(*s, dtype=torch.bfloat16) * 0.4)(H * Dh, D) * ((H * Dh) ** -0.5)),
        }
    _A_in = _A_setup()

    def _B_setup():
        B, S, D, F = 2, 1, 640, 1536
        return {
            "x": _w((lambda *s: torch.randn(*s, dtype=torch.bfloat16) * 0.05)(B, S, D)),
            "w1": _w((lambda *s: torch.randn(*s, dtype=torch.bfloat16) * 0.05)(D, F)),
            "b1": _w((lambda *s: torch.randn(*s, dtype=torch.bfloat16) * 0.8)(F)),
            "w2": _w((lambda *s: torch.randn(*s, dtype=torch.bfloat16) * 0.05)(F, D)),
            "b2": _w((lambda *s: torch.randn(*s, dtype=torch.bfloat16) * 3.2)(D)),
        }
    _B_in = _B_setup()

    return {
        "A_x": _A_in["x"],
        "A_k_cache": _A_in["k_cache"],
        "A_v_cache": _A_in["v_cache"],
        "A_wq": _A_in["wq"],
        "A_wo": _A_in["wo"],
        "B_x": _B_in["x"],
        "B_w1": _B_in["w1"],
        "B_b1": _B_in["b1"],
        "B_w2": _B_in["w2"],
        "B_b2": _B_in["b2"],
    }

def reference(A_x, A_k_cache, A_v_cache, A_wq, A_wo, B_x, B_w1, B_b1, B_w2, B_b2):
    def _A_ref(x, k_cache, v_cache, wq, wo):
        B, Sq, D = x.shape
        H, Dh = 32, 192
        q = (lambda x, *d: x.permute(*d))((x @ wq).reshape(B, Sq, H, Dh), 0, 2, 1, 3)
        scores = torch.einsum("bhqd,bhkd->bhqk", q, k_cache) * (Dh ** -0.5)
        p = _softmax(scores, axis=-1)
        o = torch.einsum("bhqk,bhkd->bhqd", p, v_cache)
        return (lambda x, *d: x.permute(*d))(o, 0, 2, 1, 3).reshape(B, Sq, H * Dh) @ wo

    def _B_ref(x, w1, b1, w2, b2):
        h = torch.einsum("bsd,df->bsf", x, w1) + b1
        zero = torch.zeros_like(h)
        r = torch.where(h > zero, h, zero)
        h = r * r
        out = torch.einsum("bsf,fd->bsd", h, w2) + b2
        return out

    x = A_x
    h = x + _A_ref(_layernorm(x, axis=-1), A_k_cache, A_v_cache, A_wq, A_wo)
    h2 = h + _B_ref(_layernorm(h, axis=-1), B_w1, B_b1, B_w2, B_b2)
    return h2
```

```cpp
#include <hip/hip_runtime.h>
#include <hip/hip_bf16.h>
#include <stdint.h>

typedef unsigned int u32;
typedef unsigned short u16;
using u32x4 = __attribute__((ext_vector_type(4))) unsigned;
using f32x4 = __attribute__((ext_vector_type(4))) float;
using f32x2 = __attribute__((ext_vector_type(2))) float;
using bf16x2v = __attribute__((ext_vector_type(2))) __bf16;

constexpr int NB = 2, DM = 640, NH = 32, SK = 8192, DH = 192, FF = 1536;
constexpr int HD = NH * DH;
constexpr int NSPLIT = 4;
constexpr int KEYS_WG = SK / NSPLIT;
constexpr int WAVES = 8;
constexpr int KEYS_WAVE = KEYS_WG / WAVES;
constexpr int NBLK = KEYS_WAVE / 64;
constexpr float SCALE = 0.07216878364870322f;
constexpr float LOG2E = 1.4426950408889634f;

constexpr size_t WS_PML = 0;
constexpr size_t WS_PO = 1024;
constexpr size_t WS_YP = WS_PO + 256 * 192;
constexpr size_t WS_H = WS_YP + 32 * 2 * 640;
constexpr size_t WS_H1 = WS_H + 2 * 640;

__device__ __forceinline__ float bf2f(u32 h) { return __builtin_bit_cast(float, h << 16); }
__device__ __forceinline__ float bflo(u32 w) { return __builtin_bit_cast(float, w << 16); }
__device__ __forceinline__ float bfhi(u32 w) { return __builtin_bit_cast(float, w & 0xffff0000u); }
__device__ __forceinline__ float rbf(float x) { return __bfloat162float(__float2bfloat16(x)); }
__device__ __forceinline__ u32 f2bf(float x) { return (u32)__builtin_bit_cast(u16, __float2bfloat16(x)); }

__device__ __forceinline__ float dot2(u32 k, u32 q, float acc) {
  return __builtin_amdgcn_fdot2_f32_bf16(__builtin_bit_cast(bf16x2v, k), __builtin_bit_cast(bf16x2v, q), acc, false);
}
template <int CTRL> __device__ __forceinline__ float dpp(float x) {
  return __builtin_bit_cast(float, __builtin_amdgcn_mov_dpp(__builtin_bit_cast(int, x), CTRL, 0xf, 0xf, true));
}
template <int CTRL, int BANK> __device__ __forceinline__ float dpp_upd(float old, float x) {
  return __builtin_bit_cast(float, __builtin_amdgcn_update_dpp(__builtin_bit_cast(int, old), __builtin_bit_cast(int, x), CTRL, 0xf, BANK, false));
}
constexpr int XOR1 = 0xB1, XOR2 = 0x4E, HMIR = 0x141, ROR8 = 0x128, ROR4 = 0x124;

__device__ __forceinline__ float wave_max(float x) {
  x = fmaxf(x, dpp<XOR1>(x));
  x = fmaxf(x, dpp<XOR2>(x));
  x = fmaxf(x, dpp<HMIR>(x));
  x = fmaxf(x, dpp<ROR8>(x));
  auto s = __builtin_amdgcn_permlane16_swap(__float_as_uint(x), __float_as_uint(x), false, false);
  x = fmaxf(__uint_as_float(s[0]), __uint_as_float(s[1]));
  auto t = __builtin_amdgcn_permlane32_swap(__float_as_uint(x), __float_as_uint(x), false, false);
  return fmaxf(__uint_as_float(t[0]), __uint_as_float(t[1]));
}
__device__ __forceinline__ float wave_sum(float x) {
  x += dpp<XOR1>(x);
  x += dpp<XOR2>(x);
  x += dpp<HMIR>(x);
  x += dpp<ROR8>(x);
  auto s = __builtin_amdgcn_permlane16_swap(__float_as_uint(x), __float_as_uint(x), false, false);
  x = __uint_as_float(s[0]) + __uint_as_float(s[1]);
  auto t = __builtin_amdgcn_permlane32_swap(__float_as_uint(x), __float_as_uint(x), false, false);
  return __uint_as_float(t[0]) + __uint_as_float(t[1]);
}
__device__ __forceinline__ void tr_reduce24(float (&a)[24], float (&o)[3]) {
  float b[12], c[6];
#pragma unroll
  for (int v = 0; v < 12; ++v) {
    auto s = __builtin_amdgcn_permlane32_swap(__float_as_uint(a[v]), __float_as_uint(a[v + 12]), false, false);
    b[v] = __uint_as_float(s[0]) + __uint_as_float(s[1]);
  }
#pragma unroll
  for (int v = 0; v < 6; ++v) {
    auto s = __builtin_amdgcn_permlane16_swap(__float_as_uint(b[v]), __float_as_uint(b[v + 6]), false, false);
    c[v] = __uint_as_float(s[0]) + __uint_as_float(s[1]);
  }
#pragma unroll
  for (int v = 0; v < 3; ++v) {
    float t = dpp_upd<ROR8, 0xC>(c[v], c[v + 3]);
    float u = dpp_upd<ROR8, 0x3>(c[v + 3], c[v]);
    o[v] = t + u;
  }
}

__device__ __forceinline__ u32x4 ldnt(const u16* p) { return __builtin_nontemporal_load(reinterpret_cast<const u32x4*>(p)); }
__device__ __forceinline__ u32x4 ld16(const u16* p) { return *reinterpret_cast<const u32x4*>(p); }

#define RING 32
constexpr int NW = 30;
constexpr int NLOADS = NW + NBLK * 48;
typedef __amdgpu_buffer_rsrc_t rsrc_t;

struct Streams { rsrc_t rw, rk, rv; int vo_w, vo_kv; };

template <int N> __device__ __forceinline__ u32x4 issue(const Streams& s) {
  if constexpr (N < NW) {
    constexpr int t = N / 3, j = N % 3;
    return __builtin_amdgcn_raw_buffer_load_b128(s.rw, s.vo_w, t * 8 * HD * 2 + j * 128, 0);
  } else {
    constexpr int n = N - NW, blk = n / 48, rem = n % 48, isv = rem / 24, q = rem % 24, i = q / 3, j = q % 3;
    constexpr int off = (blk * 64 + i * 8) * DH * 2 + j * 128;
    if constexpr (isv) return __builtin_amdgcn_raw_buffer_load_b128(s.rv, s.vo_kv, off, 2);
    else return __builtin_amdgcn_raw_buffer_load_b128(s.rk, s.vo_kv, off, 2);
  }
}
template <int N> __device__ __forceinline__ void refill(u32x4 (&ring)[RING], const Streams& s) {
  if constexpr (N + RING < NLOADS) ring[N % RING] = issue<N + RING>(s);
  __builtin_amdgcn_sched_barrier(0);
}
template <int N, int E> __device__ __forceinline__ void prologue(u32x4 (&ring)[RING], const Streams& s) {
  if constexpr (N < E) { ring[N] = issue<N>(s); prologue<N + 1, E>(ring, s); }
}
__device__ __forceinline__ void fma8(float* acc, const u32x4 w, const float p) {
#pragma unroll
  for (int k = 0; k < 4; ++k) {
    acc[2 * k] = fmaf(p, bflo(w[k]), acc[2 * k]);
    acc[2 * k + 1] = fmaf(p, bfhi(w[k]), acc[2 * k + 1]);
  }
}
template <int T> __device__ __forceinline__ void qproj(u32x4 (&ring)[RING], const Streams& s, float (&acc)[24], const float (&xn)[10]) {
  if constexpr (T < 10) {
    fma8(acc + 0, ring[(T * 3 + 0) % RING], xn[T]);  refill<T * 3 + 0>(ring, s);
    fma8(acc + 8, ring[(T * 3 + 1) % RING], xn[T]);  refill<T * 3 + 1>(ring, s);
    fma8(acc + 16, ring[(T * 3 + 2) % RING], xn[T]); refill<T * 3 + 2>(ring, s);
    qproj<T + 1>(ring, s, acc, xn);
  }
}
template <int BASE, int I> __device__ __forceinline__ void qk(u32x4 (&ring)[RING], const Streams& s, float (&xs)[8], const u32x4 (&qf)[3]) {
  if constexpr (I < 8) {
    float d = 0.f;
#pragma unroll
    for (int k = 0; k < 4; ++k) d = dot2(ring[(BASE + I * 3 + 0) % RING][k], qf[0][k], d);
    refill<BASE + I * 3 + 0>(ring, s);
#pragma unroll
    for (int k = 0; k < 4; ++k) d = dot2(ring[(BASE + I * 3 + 1) % RING][k], qf[1][k], d);
    refill<BASE + I * 3 + 1>(ring, s);
#pragma unroll
    for (int k = 0; k < 4; ++k) d = dot2(ring[(BASE + I * 3 + 2) % RING][k], qf[2][k], d);
    refill<BASE + I * 3 + 2>(ring, s);
    xs[I] = d;
    qk<BASE, I + 1>(ring, s, xs, qf);
  }
}
template <int BASE, int I> __device__ __forceinline__ void pv(u32x4 (&ring)[RING], const Streams& s, float (&acc)[24], const float (&pb)[8]) {
  if constexpr (I < 8) {
    fma8(acc + 0, ring[(BASE + I * 3 + 0) % RING], pb[I]);  refill<BASE + I * 3 + 0>(ring, s);
    fma8(acc + 8, ring[(BASE + I * 3 + 1) % RING], pb[I]);  refill<BASE + I * 3 + 1>(ring, s);
    fma8(acc + 16, ring[(BASE + I * 3 + 2) % RING], pb[I]); refill<BASE + I * 3 + 2>(ring, s);
    pv<BASE, I + 1>(ring, s, acc, pb);
  }
}
template <int BLK> __device__ __forceinline__ void blocks(u32x4 (&ring)[RING], const Streams& s, float (&acc)[24], const u32x4 (&qf)[3],
                                                          float& m, float& l, const int c) {
  if constexpr (BLK < NBLK) {
    constexpr int BASE = NW + BLK * 48;
    float xsx[8];
    qk<BASE, 0>(ring, s, xsx, qf);
    float y[4], z[2];
#pragma unroll
    for (int i = 0; i < 4; ++i) {
      const float t = xsx[i] + dpp<HMIR>(xsx[i]), u = xsx[i + 4] + dpp<HMIR>(xsx[i + 4]);
      y[i] = (c & 4) ? u : t;
    }
#pragma unroll
    for (int i = 0; i < 2; ++i) {
      const float t = y[i] + dpp<XOR2>(y[i]), u = y[i + 2] + dpp<XOR2>(y[i + 2]);
      z[i] = (c & 2) ? u : t;
    }
    float sc;
    {
      const float t = z[0] + dpp<XOR1>(z[0]), u = z[1] + dpp<XOR1>(z[1]);
      sc = (c & 1) ? u : t;
    }
    sc = rbf(rbf(sc) * SCALE);
    const float bm = wave_max(sc);
    const float mn = fmaxf(m, bm);
    const float alpha = __builtin_amdgcn_exp2f((m - mn) * LOG2E);
    const float p = __builtin_amdgcn_exp2f((sc - mn) * LOG2E);
    l = fmaf(l, alpha, p);
    m = mn;
    if constexpr (BLK > 0) {
#pragma unroll
      for (int n = 0; n < 24; ++n) acc[n] *= alpha;
    }
    float pb[8];
    {
      const float a0 = dpp<0x00>(p), a1 = dpp<0x55>(p), a2 = dpp<0xAA>(p), a3 = dpp<0xFF>(p);
      pb[0] = dpp_upd<HMIR, 0xA>(a0, a0); pb[4] = dpp_upd<HMIR, 0x5>(a0, a0);
      pb[1] = dpp_upd<HMIR, 0xA>(a1, a1); pb[5] = dpp_upd<HMIR, 0x5>(a1, a1);
      pb[2] = dpp_upd<HMIR, 0xA>(a2, a2); pb[6] = dpp_upd<HMIR, 0x5>(a2, a2);
      pb[3] = dpp_upd<HMIR, 0xA>(a3, a3); pb[7] = dpp_upd<HMIR, 0x5>(a3, a3);
    }
    pv<BASE + 24, 0>(ring, s, acc, pb);
    blocks<BLK + 1>(ring, s, acc, qf, m, l, c);
  }
}

__global__ void __launch_bounds__(512, 2)
attn_kernel(const u16* __restrict__ x, const u16* __restrict__ kc, const u16* __restrict__ vc,
            const u16* __restrict__ wq, float* __restrict__ ws) {
  __shared__ __attribute__((aligned(16))) float s_red[WAVES][DH];
  __shared__ __attribute__((aligned(16))) u16 s_q[DH];
  __shared__ float s_m[WAVES], s_l[WAVES];

  const int bid = blockIdx.x;
  const int xcd = bid & 7, jj = bid >> 3;
  const int h = xcd * 4 + (jj >> 3);
  const int b = (jj >> 2) & 1, sp = jj & 3;
  const int tid = threadIdx.x, lane = tid & 63;
  const int wave = __builtin_amdgcn_readfirstlane(tid >> 6);
  const int r = lane >> 3, c = lane & 7;

  const u16* xb = x + b * DM;
  u32 xs[5];
#pragma unroll
  for (int i = 0; i < 5; ++i) xs[i] = reinterpret_cast<const u32*>(xb)[lane + 64 * i];
  u32 xr[10];
#pragma unroll
  for (int t = 0; t < 10; ++t) xr[t] = xb[80 * wave + 8 * t + r];

  Streams s;
  {
    const u16* wqp = wq + (size_t)(80 * wave) * HD + h * DH;
    const size_t kvoff = ((size_t)(b * NH + h) * SK + sp * KEYS_WG + wave * KEYS_WAVE) * DH;
    s.rw = __builtin_amdgcn_make_buffer_rsrc((void*)wqp, 0, (79 * HD + DH) * 2, 0x00020000);
    s.rk = __builtin_amdgcn_make_buffer_rsrc((void*)(kc + kvoff), 0, KEYS_WAVE * DH * 2, 0x00020000);
    s.rv = __builtin_amdgcn_make_buffer_rsrc((void*)(vc + kvoff), 0, KEYS_WAVE * DH * 2, 0x00020000);
    s.vo_w = r * HD * 2 + c * 16;
    s.vo_kv = r * DH * 2 + c * 16;
  }
  u32x4 ring[RING];
  __builtin_amdgcn_sched_barrier(0);
  prologue<0, RING>(ring, s);
  __builtin_amdgcn_sched_barrier(0);

  float sum = 0.f;
#pragma unroll
  for (int i = 0; i < 5; ++i) sum += bflo(xs[i]) + bfhi(xs[i]);
  sum = wave_sum(sum);
  const float mean = sum * (1.0f / DM);
  float sq = 0.f;
#pragma unroll
  for (int i = 0; i < 5; ++i) { float a = bflo(xs[i]) - mean, bb = bfhi(xs[i]) - mean; sq += a * a + bb * bb; }
  sq = wave_sum(sq);
  const float mb = rbf(mean), vb = rbf(sq * (1.0f / DM));
  const float den = rbf(sqrtf(rbf(vb + 1e-5f)));
  float xn[10];
#pragma unroll
  for (int t = 0; t < 10; ++t) xn[t] = rbf(rbf(bf2f(xr[t]) - mb) / den);

  float acc[24];
#pragma unroll
  for (int n = 0; n < 24; ++n) acc[n] = 0.f;
  qproj<0>(ring, s, acc, xn);

  const int n0 = 12 * (lane >> 5) + 6 * ((lane >> 4) & 1) + 3 * ((lane >> 3) & 1);
  {
    float o3[3];
    tr_reduce24(acc, o3);
#pragma unroll
    for (int v = 0; v < 3; ++v) { const int n = n0 + v; s_red[wave][(n >> 3) * 64 + c * 8 + (n & 7)] = o3[v]; }
  }
  __syncthreads();
  if (tid < DH) {
    float q = 0.f;
#pragma unroll
    for (int w = 0; w < WAVES; ++w) q += s_red[w][tid];
    s_q[tid] = (u16)f2bf(q);
  }
  __syncthreads();
  u32x4 qf[3];
#pragma unroll
  for (int j = 0; j < 3; ++j) qf[j] = *reinterpret_cast<const u32x4*>(&s_q[j * 64 + c * 8]);

  float m = -INFINITY, l = 0.f;
#pragma unroll
  for (int n = 0; n < 24; ++n) acc[n] = 0.f;
  blocks<0>(ring, s, acc, qf, m, l, c);

  l = wave_sum(l);
  {
    float o3[3];
    tr_reduce24(acc, o3);
#pragma unroll
    for (int v = 0; v < 3; ++v) { const int n = n0 + v; s_red[wave][(n >> 3) * 64 + c * 8 + (n & 7)] = o3[v]; }
    if (lane == 0) { s_m[wave] = m; s_l[wave] = l; }
  }
  __syncthreads();
  if (tid < DH) {
    float M = s_m[0];
#pragma unroll
    for (int w = 1; w < WAVES; ++w) M = fmaxf(M, s_m[w]);
    float L = 0.f, o = 0.f;
#pragma unroll
    for (int w = 0; w < WAVES; ++w) {
      const float e = __builtin_amdgcn_exp2f((s_m[w] - M) * LOG2E);
      L = fmaf(e, s_l[w], L);
      o = fmaf(e, s_red[w][tid], o);
    }
    const int slot = (b * NH + h) * NSPLIT + sp;
    ws[WS_PO + (size_t)slot * DH + tid] = o;
    if (tid == 0) { ws[WS_PML + slot * 2] = M; ws[WS_PML + slot * 2 + 1] = L; }
  }
}

__global__ void __launch_bounds__(256)
wo_kernel(const u16* __restrict__ wo, float* __restrict__ ws) {
  __shared__ float s_o[2][DH];
  __shared__ float s_part[24][2][80];
  const int h = blockIdx.x >> 3, g = blockIdx.x & 7;
  const int tid = threadIdx.x;
  const int rg = tid / 10, cc = tid % 10;
  u32x4 w[8];
  if (tid < 240) {
#pragma unroll
    for (int t = 0; t < 8; ++t) w[t] = ld16(wo + (size_t)(h * DH + rg + 24 * t) * DM + g * 80 + cc * 8);
  }
  for (int e = tid; e < 2 * DH; e += 256) {
    const int b = e / DH, d = e % DH;
    const int slot = (b * NH + h) * NSPLIT;
    float M = -INFINITY;
#pragma unroll
    for (int s = 0; s < NSPLIT; ++s) M = fmaxf(M, ws[WS_PML + (slot + s) * 2]);
    float L = 0.f, o = 0.f;
#pragma unroll
    for (int s = 0; s < NSPLIT; ++s) {
      const float ew = __builtin_amdgcn_exp2f((ws[WS_PML + (slot + s) * 2] - M) * LOG2E);
      L = fmaf(ew, ws[WS_PML + (slot + s) * 2 + 1], L);
      o = fmaf(ew, ws[WS_PO + (size_t)(slot + s) * DH + d], o);
    }
    s_o[b][d] = rbf(o / L);
  }
  __syncthreads();
  if (tid < 240) {
    float a0[8], a1[8];
#pragma unroll
    for (int e = 0; e < 8; ++e) { a0[e] = 0.f; a1[e] = 0.f; }
#pragma unroll
    for (int t = 0; t < 8; ++t) {
      const float o0 = s_o[0][rg + 24 * t], o1 = s_o[1][rg + 24 * t];
#pragma unroll
      for (int k = 0; k < 4; ++k) {
        a0[2 * k] = fmaf(o0, bflo(w[t][k]), a0[2 * k]); a0[2 * k + 1] = fmaf(o0, bfhi(w[t][k]), a0[2 * k + 1]);
        a1[2 * k] = fmaf(o1, bflo(w[t][k]), a1[2 * k]); a1[2 * k + 1] = fmaf(o1, bfhi(w[t][k]), a1[2 * k + 1]);
      }
    }
#pragma unroll
    for (int e = 0; e < 8; ++e) { s_part[rg][0][cc * 8 + e] = a0[e]; s_part[rg][1][cc * 8 + e] = a1[e]; }
  }
  __syncthreads();
  if (tid < 160) {
    const int b = tid / 80, col = tid % 80;
    float y = 0.f;
#pragma unroll
    for (int q = 0; q < 24; ++q) y += s_part[q][b][col];
    ws[WS_YP + ((size_t)h * 2 + b) * DM + g * 80 + col] = y;
  }
}

__global__ void __launch_bounds__(256)
mlp1_kernel(const u16* __restrict__ x, const u16* __restrict__ w1, const u16* __restrict__ b1, float* __restrict__ ws) {
  __shared__ float s_h[2][DM];
  __shared__ float s_r[8];
  __shared__ float s_part[4][2][8];
  const int tid = threadIdx.x, lane = tid & 63, wave = tid >> 6;
  const int f0 = blockIdx.x * 8;
  u32x4 w[3];
#pragma unroll
  for (int t = 0; t < 3; ++t) { const int row = tid + 256 * t; if (row < DM) w[t] = ld16(w1 + (size_t)row * FF + f0); else w[t] = u32x4{0, 0, 0, 0}; }
  for (int e = tid; e < 2 * DM; e += 256) {
    float y = 0.f;
#pragma unroll 8
    for (int hh = 0; hh < NH; ++hh) y += ws[WS_YP + (size_t)hh * 2 * DM + e];
    const float hv = rbf(bf2f(x[e]) + rbf(y));
    s_h[e / DM][e % DM] = hv;
    if (blockIdx.x == 0) ws[WS_H + e] = hv;
  }
  __syncthreads();
  if (wave < 2) {
    float s = 0.f;
    for (int i = lane; i < DM; i += 64) s += s_h[wave][i];
    s = wave_sum(s);
    const float mean = s * (1.0f / DM);
    float q = 0.f;
    for (int i = lane; i < DM; i += 64) { const float d = s_h[wave][i] - mean; q += d * d; }
    q = wave_sum(q);
    if (lane == 0) { s_r[wave * 2] = rbf(mean); s_r[wave * 2 + 1] = rbf(sqrtf(rbf(rbf(q * (1.0f / DM)) + 1e-5f))); }
  }
  __syncthreads();
  float a0[8], a1[8];
#pragma unroll
  for (int e = 0; e < 8; ++e) { a0[e] = 0.f; a1[e] = 0.f; }
#pragma unroll
  for (int t = 0; t < 3; ++t) {
    const int row = tid + 256 * t;
    if (row < DM) {
      const float n0 = rbf(rbf(s_h[0][row] - s_r[0]) / s_r[1]);
      const float n1 = rbf(rbf(s_h[1][row] - s_r[2]) / s_r[3]);
#pragma unroll
      for (int k = 0; k < 4; ++k) {
        a0[2 * k] = fmaf(n0, bflo(w[t][k]), a0[2 * k]); a0[2 * k + 1] = fmaf(n0, bfhi(w[t][k]), a0[2 * k + 1]);
        a1[2 * k] = fmaf(n1, bflo(w[t][k]), a1[2 * k]); a1[2 * k + 1] = fmaf(n1, bfhi(w[t][k]), a1[2 * k + 1]);
      }
    }
  }
#pragma unroll
  for (int e = 0; e < 8; ++e) { a0[e] = wave_sum(a0[e]); a1[e] = wave_sum(a1[e]); }
  if (lane == 0) {
#pragma unroll
    for (int e = 0; e < 8; ++e) { s_part[wave][0][e] = a0[e]; s_part[wave][1][e] = a1[e]; }
  }
  __syncthreads();
  if (tid < 16) {
    const int b = tid >> 3, e = tid & 7;
    const float s = s_part[0][b][e] + s_part[1][b][e] + s_part[2][b][e] + s_part[3][b][e];
    const float hh = rbf(rbf(s) + bf2f(b1[f0 + e]));
    const float rr = hh > 0.f ? hh : 0.f;
    ws[WS_H1 + b * FF + f0 + e] = rbf(rr * rr);
  }
}

__global__ void __launch_bounds__(256)
mlp2_kernel(const u16* __restrict__ w2, const u16* __restrict__ b2, const float* __restrict__ ws, u16* __restrict__ out) {
  __shared__ float s_part[4][2][8];
  const int tid = threadIdx.x, lane = tid & 63, wave = tid >> 6;
  const int d0 = blockIdx.x * 8;
  u32x4 w[6];
#pragma unroll
  for (int t = 0; t < 6; ++t) w[t] = ld16(w2 + (size_t)(tid + 256 * t) * DM + d0);
  float a0[8], a1[8];
#pragma unroll
  for (int e = 0; e < 8; ++e) { a0[e] = 0.f; a1[e] = 0.f; }
#pragma unroll
  for (int t = 0; t < 6; ++t) {
    const int row = tid + 256 * t;
    const float n0 = ws[WS_H1 + row], n1 = ws[WS_H1 + FF + row];
#pragma unroll
    for (int k = 0; k < 4; ++k) {
      a0[2 * k] = fmaf(n0, bflo(w[t][k]), a0[2 * k]); a0[2 * k + 1] = fmaf(n0, bfhi(w[t][k]), a0[2 * k + 1]);
      a1[2 * k] = fmaf(n1, bflo(w[t][k]), a1[2 * k]); a1[2 * k + 1] = fmaf(n1, bfhi(w[t][k]), a1[2 * k + 1]);
    }
  }
#pragma unroll
  for (int e = 0; e < 8; ++e) { a0[e] = wave_sum(a0[e]); a1[e] = wave_sum(a1[e]); }
  if (lane == 0) {
#pragma unroll
    for (int e = 0; e < 8; ++e) { s_part[wave][0][e] = a0[e]; s_part[wave][1][e] = a1[e]; }
  }
  __syncthreads();
  if (tid < 16) {
    const int b = tid >> 3, e = tid & 7;
    const float s = s_part[0][b][e] + s_part[1][b][e] + s_part[2][b][e] + s_part[3][b][e];
    const float o = rbf(rbf(s) + bf2f(b2[d0 + e]));
    out[b * DM + d0 + e] = (u16)f2bf(ws[WS_H + b * DM + d0 + e] + o);
  }
}

extern "C" void kernel_launch(void* const* d_in, const int* in_sizes, int n_in,
                              void* d_out, int out_size, void* d_ws, size_t ws_size,
                              hipStream_t stream) {
  const u16* x = (const u16*)d_in[0];
  const u16* kc = (const u16*)d_in[1];
  const u16* vc = (const u16*)d_in[2];
  const u16* wq = (const u16*)d_in[3];
  const u16* wo = (const u16*)d_in[4];
  const u16* w1 = (const u16*)d_in[6];
  const u16* b1 = (const u16*)d_in[7];
  const u16* w2 = (const u16*)d_in[8];
  const u16* b2 = (const u16*)d_in[9];
  float* ws = (float*)d_ws;
  attn_kernel<<<256, 512, 0, stream>>>(x, kc, vc, wq, ws);
  wo_kernel<<<256, 256, 0, stream>>>(wo, ws);
  mlp1_kernel<<<192, 256, 0, stream>>>(x, w1, b1, ws);
  mlp2_kernel<<<80, 256, 0, stream>>>(w2, b2, ws, (u16*)d_out);
}
```

```cpp
#include <hip/hip_runtime.h>
#include <hip/hip_bf16.h>
#include <stdint.h>

typedef unsigned int u32;
typedef unsigned short u16;
typedef unsigned long long u64;
using u32x4 = __attribute__((ext_vector_type(4))) unsigned;
using u32x2 = __attribute__((ext_vector_type(2))) unsigned;
using f32x2 = __attribute__((ext_vector_type(2))) float;
using bf16x2v = __attribute__((ext_vector_type(2))) __bf16;
typedef __amdgpu_buffer_rsrc_t rsrc_t;

constexpr int DM = 640, NH = 32, SK = 8192, DH = 192, FF = 1536;
constexpr int HD = NH * DH;
constexpr int NSPLIT = 4;
constexpr int KEYS_WG = SK / NSPLIT;
constexpr int WAVES = 8;
constexpr int KEYS_WAVE = KEYS_WG / WAVES;
constexpr int NBLK = KEYS_WAVE / 64;
constexpr float SCALE = 0.07216878364870322f;
constexpr float LOG2E = 1.4426950408889634f;

#define RING 16
constexpr int NW = 30;
constexpr int NLOADS = NW + NBLK * 48;

constexpr u32 OFF_EPOCH = 0;
constexpr u32 OFF_FLAGS = 1024;
constexpr u32 OFF_H1 = 4096;
constexpr u32 H1_STRIDE = 1664;
constexpr u32 OFF_H2 = OFF_H1 + 256 * H1_STRIDE;
constexpr u32 OFF_H3 = OFF_H2 + 8 * 2 * DM * 8;
constexpr u32 WS_BYTES = OFF_H3 + FF * 8;
constexpr u32 TAG = 0x5bd1e995u;
constexpr int SPIN_MAX = 1 << 18;
constexpr int AUX_NT = 2, AUX_SC1 = 16;

__device__ __forceinline__ float bf2f(u32 h) { return __builtin_bit_cast(float, h << 16); }
__device__ __forceinline__ float bflo(u32 w) { return __builtin_bit_cast(float, w << 16); }
__device__ __forceinline__ float bfhi(u32 w) { return __builtin_bit_cast(float, w & 0xffff0000u); }
__device__ __forceinline__ float rbf(float x) { return __bfloat162float(__float2bfloat16(x)); }
__device__ __forceinline__ u32 f2bf(float x) { return (u32)__builtin_bit_cast(u16, __float2bfloat16(x)); }
__device__ __forceinline__ u32 fbits(float x) { return __builtin_bit_cast(u32, x); }
__device__ __forceinline__ float bitsf(u32 x) { return __builtin_bit_cast(float, x); }

__device__ __forceinline__ float dot2(u32 k, u32 q, float acc) {
  return __builtin_amdgcn_fdot2_f32_bf16(__builtin_bit_cast(bf16x2v, k), __builtin_bit_cast(bf16x2v, q), acc, false);
}
template <int CTRL> __device__ __forceinline__ float dpp(float x) {
  return __builtin_bit_cast(float, __builtin_amdgcn_mov_dpp(__builtin_bit_cast(int, x), CTRL, 0xf, 0xf, true));
}
template <int CTRL, int BANK> __device__ __forceinline__ float dpp_upd(float old, float x) {
  return __builtin_bit_cast(float, __builtin_amdgcn_update_dpp(__builtin_bit_cast(int, old), __builtin_bit_cast(int, x), CTRL, 0xf, BANK, false));
}
constexpr int XOR1 = 0xB1, XOR2 = 0x4E, HMIR = 0x141, ROR8 = 0x128;

__device__ __forceinline__ float wave_max(float x) {
  x = fmaxf(x, dpp<XOR1>(x));
  x = fmaxf(x, dpp<XOR2>(x));
  x = fmaxf(x, dpp<HMIR>(x));
  x = fmaxf(x, dpp<ROR8>(x));
  auto s = __builtin_amdgcn_permlane16_swap(__float_as_uint(x), __float_as_uint(x), false, false);
  x = fmaxf(__uint_as_float(s[0]), __uint_as_float(s[1]));
  auto t = __builtin_amdgcn_permlane32_swap(__float_as_uint(x), __float_as_uint(x), false, false);
  return fmaxf(__uint_as_float(t[0]), __uint_as_float(t[1]));
}
__device__ __forceinline__ float wave_sum(float x) {
  x += dpp<XOR1>(x);
  x += dpp<XOR2>(x);
  x += dpp<HMIR>(x);
  x += dpp<ROR8>(x);
  auto s = __builtin_amdgcn_permlane16_swap(__float_as_uint(x), __float_as_uint(x), false, false);
  x = __uint_as_float(s[0]) + __uint_as_float(s[1]);
  auto t = __builtin_amdgcn_permlane32_swap(__float_as_uint(x), __float_as_uint(x), false, false);
  return __uint_as_float(t[0]) + __uint_as_float(t[1]);
}
__device__ __forceinline__ float red32(float a, float b) {
  auto s = __builtin_amdgcn_permlane32_swap(__float_as_uint(a), __float_as_uint(b), false, false);
  return __uint_as_float(s[0]) + __uint_as_float(s[1]);
}
__device__ __forceinline__ float red16(float a, float b) {
  auto s = __builtin_amdgcn_permlane16_swap(__float_as_uint(a), __float_as_uint(b), false, false);
  return __uint_as_float(s[0]) + __uint_as_float(s[1]);
}
__device__ __forceinline__ float red8(float a, float b) { return dpp_upd<ROR8, 0xC>(a, b) + dpp_upd<ROR8, 0x3>(b, a); }
__device__ __forceinline__ float red4m(float a, float b) { return dpp_upd<HMIR, 0xA>(a, b) + dpp_upd<HMIR, 0x5>(b, a); }
__device__ __forceinline__ float red2(float a, float b, int lane) { const float t = a + dpp<XOR2>(a), u = b + dpp<XOR2>(b); return (lane & 2) ? u : t; }
__device__ __forceinline__ float red1(float a, float b, int lane) { const float t = a + dpp<XOR1>(a), u = b + dpp<XOR1>(b); return (lane & 1) ? u : t; }

__device__ __forceinline__ void tr_reduce24(float (&a)[24], float (&o)[3]) {
  float b[12], c[6];
#pragma unroll
  for (int v = 0; v < 12; ++v) b[v] = red32(a[v], a[v + 12]);
#pragma unroll
  for (int v = 0; v < 6; ++v) c[v] = red16(b[v], b[v + 6]);
#pragma unroll
  for (int v = 0; v < 3; ++v) o[v] = red8(c[v], c[v + 3]);
}

struct Streams { rsrc_t rw, rk, rv; int vo_w, vo_kv; };

template <int N> __device__ __forceinline__ u32x4 issue(const Streams& s) {
  if constexpr (N < NW) {
    constexpr int t = N / 3, j = N % 3;
    return __builtin_amdgcn_raw_buffer_load_b128(s.rw, s.vo_w, t * 8 * HD * 2 + j * 128, AUX_NT);
  } else {
    constexpr int n = N - NW, blk = n / 48, rem = n % 48, isv = rem / 24, q = rem % 24, i = q / 3, j = q % 3;
    constexpr int off = (blk * 64 + i * 8) * DH * 2 + j * 128;
    if constexpr (isv) return __builtin_amdgcn_raw_buffer_load_b128(s.rv, s.vo_kv, off, AUX_NT);
    else return __builtin_amdgcn_raw_buffer_load_b128(s.rk, s.vo_kv, off, AUX_NT);
  }
}
struct Tail {
  rsrc_t rwo, rw1, rw2; int vo_wo, vo_1, vo_2; bool role1;
  const u64* fp; u64 flag;
  u32x2 wo[12]; u32x4 w[3]; u32x2 w2[3];
};
__device__ __forceinline__ void issue_tail(Tail& t) {
#pragma unroll
  for (int i = 0; i < 12; ++i) t.wo[i] = __builtin_amdgcn_raw_buffer_load_b64(t.rwo, t.vo_wo, i * 8 * DM * 2, AUX_NT);
#pragma unroll
  for (int i = 0; i < 3; ++i) t.w[i] = __builtin_amdgcn_raw_buffer_load_b128(t.rw1, t.vo_1, i * 256 * FF * 2, AUX_NT);
#pragma unroll
  for (int i = 0; i < 3; ++i) t.w2[i] = __builtin_amdgcn_raw_buffer_load_b64(t.rw2, t.vo_2, i * 512 * DM * 2, AUX_NT);
}
template <int N> __device__ __forceinline__ void refill(u32x4 (&ring)[RING], const Streams& s, Tail& t) {
  if constexpr (N + RING < NLOADS) ring[N % RING] = issue<N + RING>(s);
  else if constexpr (N + RING == NLOADS) issue_tail(t);
  if constexpr (N == NW + 2 * 48) t.flag = __hip_atomic_load(t.fp, __ATOMIC_RELAXED, __HIP_MEMORY_SCOPE_AGENT);
  __builtin_amdgcn_sched_barrier(0);
}
template <int N, int E> __device__ __forceinline__ void prologue(u32x4 (&ring)[RING], const Streams& s) {
  if constexpr (N < E) { ring[N] = issue<N>(s); prologue<N + 1, E>(ring, s); }
}
__device__ __forceinline__ void fma8(float* acc, const u32x4 w, const float p) {
#pragma unroll
  for (int k = 0; k < 4; ++k) {
    acc[2 * k] = fmaf(p, bflo(w[k]), acc[2 * k]);
    acc[2 * k + 1] = fmaf(p, bfhi(w[k]), acc[2 * k + 1]);
  }
}
template <int T> __device__ __forceinline__ void qproj(u32x4 (&ring)[RING], const Streams& s, Tail& tl, float (&acc)[24], const float (&xn)[10]) {
  if constexpr (T < 10) {
    fma8(acc + 0, ring[(T * 3 + 0) % RING], xn[T]);  refill<T * 3 + 0>(ring, s, tl);
    fma8(acc + 8, ring[(T * 3 + 1) % RING], xn[T]);  refill<T * 3 + 1>(ring, s, tl);
    fma8(acc + 16, ring[(T * 3 + 2) % RING], xn[T]); refill<T * 3 + 2>(ring, s, tl);
    qproj<T + 1>(ring, s, tl, acc, xn);
  }
}
template <int BASE, int I> __device__ __forceinline__ void qk(u32x4 (&ring)[RING], const Streams& s, Tail& tl, float (&xs)[8], const u32x4 (&qf)[3]) {
  if constexpr (I < 8) {
    float d = 0.f;
#pragma unroll
    for (int k = 0; k < 4; ++k) d = dot2(ring[(BASE + I * 3 + 0) % RING][k], qf[0][k], d);
    refill<BASE + I * 3 + 0>(ring, s, tl);
#pragma unroll
    for (int k = 0; k < 4; ++k) d = dot2(ring[(BASE + I * 3 + 1) % RING][k], qf[1][k], d);
    refill<BASE + I * 3 + 1>(ring, s, tl);
#pragma unroll
    for (int k = 0; k < 4; ++k) d = dot2(ring[(BASE + I * 3 + 2) % RING][k], qf[2][k], d);
    refill<BASE + I * 3 + 2>(ring, s, tl);
    xs[I] = d;
    qk<BASE, I + 1>(ring, s, tl, xs, qf);
  }
}
template <int BASE, int I> __device__ __forceinline__ void pv(u32x4 (&ring)[RING], const Streams& s, Tail& tl, float (&acc)[24], const float (&pb)[8]) {
  if constexpr (I < 8) {
    fma8(acc + 0, ring[(BASE + I * 3 + 0) % RING], pb[I]);  refill<BASE + I * 3 + 0>(ring, s, tl);
    fma8(acc + 8, ring[(BASE + I * 3 + 1) % RING], pb[I]);  refill<BASE + I * 3 + 1>(ring, s, tl);
    fma8(acc + 16, ring[(BASE + I * 3 + 2) % RING], pb[I]); refill<BASE + I * 3 + 2>(ring, s, tl);
    pv<BASE, I + 1>(ring, s, tl, acc, pb);
  }
}
template <int BLK> __device__ __forceinline__ void blocks(u32x4 (&ring)[RING], const Streams& s, Tail& tl, float (&acc)[24], const u32x4 (&qf)[3],
                                                          float& m, float& l, const int c) {
  if constexpr (BLK < NBLK) {
    constexpr int BASE = NW + BLK * 48;
    float xsx[8];
    qk<BASE, 0>(ring, s, tl, xsx, qf);
    float y[4], z[2];
#pragma unroll
    for (int i = 0; i < 4; ++i) y[i] = red4m(xsx[i], xsx[i + 4]);
#pragma unroll
    for (int i = 0; i < 2; ++i) z[i] = red2(y[i], y[i + 2], c);
    float sc = red1(z[0], z[1], c);
    sc = rbf(rbf(sc) * SCALE);
    const float bm = wave_max(sc);
    const float mn = fmaxf(m, bm);
    const float alpha = __builtin_amdgcn_exp2f((m - mn) * LOG2E);
    const float p = __builtin_amdgcn_exp2f((sc - mn) * LOG2E);
    l = fmaf(l, alpha, p);
    m = mn;
    if constexpr (BLK > 0) {
#pragma unroll
      for (int n = 0; n < 24; ++n) acc[n] *= alpha;
    }
    float pb[8];
    {
      const float a0 = dpp<0x00>(p), a1 = dpp<0x55>(p), a2 = dpp<0xAA>(p), a3 = dpp<0xFF>(p);
      pb[0] = dpp_upd<HMIR, 0xA>(a0, a0); pb[4] = dpp_upd<HMIR, 0x5>(a0, a0);
      pb[1] = dpp_upd<HMIR, 0xA>(a1, a1); pb[5] = dpp_upd<HMIR, 0x5>(a1, a1);
      pb[2] = dpp_upd<HMIR, 0xA>(a2, a2); pb[6] = dpp_upd<HMIR, 0x5>(a2, a2);
      pb[3] = dpp_upd<HMIR, 0xA>(a3, a3); pb[7] = dpp_upd<HMIR, 0x5>(a3, a3);
    }
    pv<BASE + 24, 0>(ring, s, tl, acc, pb);
    blocks<BLK + 1>(ring, s, tl, acc, qf, m, l, c);
  }
}

__device__ __forceinline__ u32x4 ld_g2(rsrc_t r, u32 off) { return __builtin_amdgcn_raw_buffer_load_b128(r, off, 0, AUX_SC1); }
__device__ __forceinline__ void st_g2(rsrc_t r, u32 off, u32x4 v) { __builtin_amdgcn_raw_buffer_store_b128(v, r, off, 0, AUX_SC1); }
__device__ __forceinline__ bool tags_ok(const u32x4 v) { return v[1] == TAG && v[3] == TAG; }
#define POLL_FENCE() asm volatile("" ::: "memory")

__global__ void __launch_bounds__(512, 2)
fused_kernel(const u16* __restrict__ x, const u16* __restrict__ kc, const u16* __restrict__ vc,
             const u16* __restrict__ wq, const u16* __restrict__ wo, const u16* __restrict__ w1, const u16* __restrict__ b1,
             const u16* __restrict__ w2, const u16* __restrict__ b2, unsigned char* __restrict__ ws, u16* __restrict__ out) {
  __shared__ __attribute__((aligned(16))) float s_red[WAVES][DH];
  __shared__ __attribute__((aligned(16))) float s_h[2][DM];
  __shared__ __attribute__((aligned(16))) float s_y[WAVES][40];
  __shared__ __attribute__((aligned(16))) u16 s_q[DH];
  __shared__ float s_m[WAVES], s_l[WAVES];

  const int bid = blockIdx.x;
  const int g = bid & 7, jj = bid >> 3;
  const int h = g * 4 + (jj >> 3);
  const int b = (jj >> 2) & 1, sp = jj & 3;
  const int tid = threadIdx.x, lane = tid & 63;
  const int wave = __builtin_amdgcn_readfirstlane(tid >> 6);
  const int r = lane >> 3, c = lane & 7;
  const bool role1 = jj < 12;

  const rsrc_t rws = __builtin_amdgcn_make_buffer_rsrc((void*)ws, 0, WS_BYTES, 0x00020000);
  const u32 my_h1 = OFF_H1 + (u32)(g * 32 + jj) * H1_STRIDE;
  if (wave == 7) {
    const u32x4 z4 = {0u, 0u, 0u, 0u};
    st_g2(rws, my_h1 + lane * 16, z4);
    if (lane < 40) st_g2(rws, my_h1 + 1024 + lane * 16, z4);
    if (lane < 20) st_g2(rws, OFF_H2 + (u32)((g * 2 + lane / 10) * DM + 20 * jj + 2 * (lane % 10)) * 8, z4);
    if (role1 && lane < 8) st_g2(rws, OFF_H3 + (u32)(192 * g + 16 * jj + 2 * lane) * 8, z4);
  }
  const u16* xb = x + b * DM;
  u32 xs[5];
#pragma unroll
  for (int i = 0; i < 5; ++i) xs[i] = reinterpret_cast<const u32*>(xb)[lane + 64 * i];
  u32 xr[10];
#pragma unroll
  for (int t = 0; t < 10; ++t) xr[t] = xb[80 * wave + 8 * t + r];

  Streams s;
  Tail tl;
  {
    const u16* wqp = wq + (size_t)(80 * wave) * HD + h * DH;
    const size_t kvoff = ((size_t)(b * NH + h) * SK + sp * KEYS_WG + wave * KEYS_WAVE) * DH;
    s.rw = __builtin_amdgcn_make_buffer_rsrc((void*)wqp, 0, (79 * HD + DH) * 2, 0x00020000);
    s.rk = __builtin_amdgcn_make_buffer_rsrc((void*)(kc + kvoff), 0, KEYS_WAVE * DH * 2, 0x00020000);
    s.rv = __builtin_amdgcn_make_buffer_rsrc((void*)(vc + kvoff), 0, KEYS_WAVE * DH * 2, 0x00020000);
    s.vo_w = r * HD * 2 + c * 16;
    s.vo_kv = r * DH * 2 + c * 16;
    tl.rwo = __builtin_amdgcn_make_buffer_rsrc((void*)(wo + (size_t)(768 * g + 96 * wave) * DM), 0, 96 * DM * 2, 0x00020000);
    tl.vo_wo = (c < 5) ? (r * DM + 20 * jj + 4 * c) * 2 : 0x40000000;
    tl.role1 = role1;
    tl.fp = reinterpret_cast<const u64*>(ws + OFF_FLAGS) + (tid & 255);
    tl.rw1 = __builtin_amdgcn_make_buffer_rsrc((void*)w1, 0, DM * FF * 2, 0x00020000);
    tl.vo_1 = role1 ? ((32 * wave + (lane & 31)) * FF + 192 * g + 16 * jj + 8 * (lane >> 5)) * 2 : 0x40000000;
    tl.rw2 = __builtin_amdgcn_make_buffer_rsrc((void*)w2, 0, FF * DM * 2, 0x00020000);
    tl.vo_2 = role1 ? 0x40000000 : (tid * DM + 80 * g + 4 * (jj - 12)) * 2;
  }
  u32x4 ring[RING];
  __builtin_amdgcn_sched_barrier(0);
  prologue<0, RING>(ring, s);
  const u64 epoch = __hip_atomic_load(reinterpret_cast<u64*>(ws + OFF_EPOCH), __ATOMIC_RELAXED, __HIP_MEMORY_SCOPE_AGENT);
  __builtin_amdgcn_sched_barrier(0);

  float xn[10];
  {
    float sum = 0.f;
#pragma unroll
    for (int i = 0; i < 5; ++i) sum += bflo(xs[i]) + bfhi(xs[i]);
    sum = wave_sum(sum);
    const float mean = sum * (1.0f / DM);
    float sq = 0.f;
#pragma unroll
    for (int i = 0; i < 5; ++i) { float a = bflo(xs[i]) - mean, bb = bfhi(xs[i]) - mean; sq += a * a + bb * bb; }
    sq = wave_sum(sq);
    const float mb = rbf(mean), vb = rbf(sq * (1.0f / DM));
    const float den = rbf(sqrtf(rbf(vb + 1e-5f)));
#pragma unroll
    for (int t = 0; t < 10; ++t) xn[t] = rbf(rbf(bf2f(xr[t]) - mb) / den);
  }
  float acc[24];
#pragma unroll
  for (int n = 0; n < 24; ++n) acc[n] = 0.f;
  qproj<0>(ring, s, tl, acc, xn);

  const u64 tag64 = epoch + 1ull;
  asm volatile("s_waitcnt vmcnt(%0)" ::"n"(RING) : "memory");
  __builtin_amdgcn_raw_buffer_store_b64(u32x2{(u32)tag64, (u32)(tag64 >> 32)}, rws, (tid == 448) ? OFF_FLAGS + bid * 8 : 0x40000000u, 0, AUX_SC1);
  __builtin_amdgcn_sched_barrier(0);

  const int n0 = 12 * (lane >> 5) + 6 * ((lane >> 4) & 1) + 3 * ((lane >> 3) & 1);
  {
    float o3[3];
    tr_reduce24(acc, o3);
#pragma unroll
    for (int v = 0; v < 3; ++v) { const int n = n0 + v; s_red[wave][(n >> 3) * 64 + c * 8 + (n & 7)] = o3[v]; }
  }
  __syncthreads();
  if (tid < DH) {
    float q = 0.f;
#pragma unroll
    for (int w = 0; w < WAVES; ++w) q += s_red[w][tid];
    s_q[tid] = (u16)f2bf(q);
  }
  __syncthreads();
  u32x4 qf[3];
#pragma unroll
  for (int j = 0; j < 3; ++j) qf[j] = *reinterpret_cast<const u32x4*>(&s_q[j * 64 + c * 8]);

  float m = -INFINITY, l = 0.f;
#pragma unroll
  for (int n = 0; n < 24; ++n) acc[n] = 0.f;
  blocks<0>(ring, s, tl, acc, qf, m, l, c);

  l = wave_sum(l);
  {
    float o3[3];
    tr_reduce24(acc, o3);
#pragma unroll
    for (int v = 0; v < 3; ++v) { const int n = n0 + v; s_red[wave][(n >> 3) * 64 + c * 8 + (n & 7)] = o3[v]; }
    if (lane == 0) { s_m[wave] = m; s_l[wave] = l; }
  }
  if (!__all(tl.flag == tag64)) {
    for (int spin = 0; spin < SPIN_MAX; ++spin) {
      const u64 f = __hip_atomic_load(tl.fp, __ATOMIC_RELAXED, __HIP_MEMORY_SCOPE_AGENT);
      if (__all(f == tag64)) break;
      __builtin_amdgcn_s_sleep(4);
    }
  }
  __syncthreads();
  if (tid <= 96) {
    float M = s_m[0];
#pragma unroll
    for (int w = 1; w < WAVES; ++w) M = fmaxf(M, s_m[w]);
    float L = 0.f, o0 = 0.f, o1 = 0.f;
    const int d = (tid < 96) ? 2 * tid : 0;
#pragma unroll
    for (int w = 0; w < WAVES; ++w) {
      const float e = __builtin_amdgcn_exp2f((s_m[w] - M) * LOG2E);
      const f32x2 a = *reinterpret_cast<const f32x2*>(&s_red[w][d]);
      L = fmaf(e, s_l[w], L);
      o0 = fmaf(e, a[0], o0);
      o1 = fmaf(e, a[1], o1);
    }
    if (tid == 96) { o0 = M; o1 = L; }
    st_g2(rws, my_h1 + tid * 16, u32x4{fbits(o0), TAG, fbits(o1), TAG});
  }
  __syncthreads();

  float* s_o = &s_red[0][0];
  {
    u32x4 pa[4], pb2[4];
    const u32 base = OFF_H1 + (u32)(g * 32 + wave * 4) * H1_STRIDE;
    const u32 oa = lane * 16, ob = (lane < 33) ? 1024 + lane * 16 : 0x40000000u;
    unsigned done = 0;
    for (int spin = 0; spin < SPIN_MAX; ++spin) {
      POLL_FENCE();
#pragma unroll
      for (int q = 0; q < 4; ++q) {
        if (!(done & (1u << q))) {
          pa[q] = ld_g2(rws, base + q * H1_STRIDE + oa);
          pb2[q] = ld_g2(rws, (lane < 33) ? base + q * H1_STRIDE + ob : ob);
        }
      }
#pragma unroll
      for (int q = 0; q < 4; ++q) {
        const bool ok = tags_ok(pa[q]) && (lane >= 33 || tags_ok(pb2[q]));
        if (__all(ok)) done |= 1u << q;
      }
      if (done == 15u) break;
      __builtin_amdgcn_s_sleep(2);
    }
    float ms[4], ls[4];
#pragma unroll
    for (int q = 0; q < 4; ++q) {
      ms[q] = bitsf(__builtin_amdgcn_readlane(pb2[q][0], 32));
      ls[q] = bitsf(__builtin_amdgcn_readlane(pb2[q][2], 32));
    }
    const float M = fmaxf(fmaxf(ms[0], ms[1]), fmaxf(ms[2], ms[3]));
    float L = 0.f, a0 = 0.f, a1 = 0.f, b0 = 0.f, b1v = 0.f;
#pragma unroll
    for (int q = 0; q < 4; ++q) {
      const float e = __builtin_amdgcn_exp2f((ms[q] - M) * LOG2E);
      L = fmaf(e, ls[q], L);
      a0 = fmaf(e, bitsf(pa[q][0]), a0); a1 = fmaf(e, bitsf(pa[q][2]), a1);
      b0 = fmaf(e, bitsf(pb2[q][0]), b0); b1v = fmaf(e, bitsf(pb2[q][2]), b1v);
    }
    const int rowb = (wave >> 1) * DH, bb = wave & 1;
    s_o[(rowb + 2 * lane) * 2 + bb] = rbf(a0 / L);
    s_o[(rowb + 2 * lane + 1) * 2 + bb] = rbf(a1 / L);
    if (lane < 32) {
      s_o[(rowb + 128 + 2 * lane) * 2 + bb] = rbf(b0 / L);
      s_o[(rowb + 128 + 2 * lane + 1) * 2 + bb] = rbf(b1v / L);
    }
  }
  __syncthreads();
  {
    float y[8];
#pragma unroll
    for (int e = 0; e < 8; ++e) y[e] = 0.f;
#pragma unroll
    for (int i = 0; i < 12; ++i) {
      const f32x2 o = *reinterpret_cast<const f32x2*>(&s_o[(96 * wave + 8 * i + r) * 2]);
      const float w0 = bflo(tl.wo[i][0]), w1v = bfhi(tl.wo[i][0]), w2v = bflo(tl.wo[i][1]), w3 = bfhi(tl.wo[i][1]);
      y[0] = fmaf(o[0], w0, y[0]); y[1] = fmaf(o[0], w1v, y[1]); y[2] = fmaf(o[0], w2v, y[2]); y[3] = fmaf(o[0], w3, y[3]);
      y[4] = fmaf(o[1], w0, y[4]); y[5] = fmaf(o[1], w1v, y[5]); y[6] = fmaf(o[1], w2v, y[6]); y[7] = fmaf(o[1], w3, y[7]);
    }
    float u[4], v[2];
#pragma unroll
    for (int e = 0; e < 4; ++e) u[e] = red32(y[e], y[e + 4]);
#pragma unroll
    for (int e = 0; e < 2; ++e) v[e] = red16(u[e], u[e + 2]);
    const float t = red8(v[0], v[1]);
    if (c < 5) s_y[wave][(lane >> 5) * 20 + 4 * c + 2 * ((lane >> 4) & 1) + ((lane >> 3) & 1)] = t;
  }
  __syncthreads();
  if (tid < 20) {
    float y0 = 0.f, y1 = 0.f;
#pragma unroll
    for (int w = 0; w < WAVES; ++w) { const f32x2 a = *reinterpret_cast<const f32x2*>(&s_y[w][2 * tid]); y0 += a[0]; y1 += a[1]; }
    st_g2(rws, OFF_H2 + (u32)((g * 2 + tid / 10) * DM + 20 * jj + 2 * (tid % 10)) * 8, u32x4{fbits(y0), TAG, fbits(y1), TAG});
  }

  if (role1) {
    const u32 xa = reinterpret_cast<const u32*>(x)[tid];
    const u32 xbv = (tid < 128) ? reinterpret_cast<const u32*>(x)[512 + tid] : 0u;
    const float bias0 = bf2f(b1[192 * g + 16 * jj + (tid & 15)]);
    {
      u32x4 pa[8], pb2[8];
      const u32 oa = OFF_H2 + tid * 16, ob = (tid < 128) ? OFF_H2 + (512 + tid) * 16 : 0x40000000u;
      for (int spin = 0; spin < SPIN_MAX; ++spin) {
        POLL_FENCE();
#pragma unroll
        for (int q = 0; q < 8; ++q) pa[q] = ld_g2(rws, oa + q * (2 * DM * 8));
        if (wave < 2) {
#pragma unroll
          for (int q = 0; q < 8; ++q) pb2[q] = ld_g2(rws, ob + q * (2 * DM * 8));
        }
        bool ok = true;
#pragma unroll
        for (int q = 0; q < 8; ++q) ok = ok && tags_ok(pa[q]);
        if (wave < 2) {
#pragma unroll
          for (int q = 0; q < 8; ++q) ok = ok && tags_ok(pb2[q]);
        }
        if (__all(ok)) break;
        __builtin_amdgcn_s_sleep(2);
      }
      float y0 = 0.f, y1 = 0.f;
#pragma unroll
      for (int q = 0; q < 8; ++q) { y0 += bitsf(pa[q][0]); y1 += bitsf(pa[q][2]); }
      float* sh = &s_h[0][0];
      *reinterpret_cast<f32x2*>(&sh[2 * tid]) = f32x2{rbf(bflo(xa) + rbf(y0)), rbf(bfhi(xa) + rbf(y1))};
      if (wave < 2) {
        y0 = 0.f; y1 = 0.f;
#pragma unroll
        for (int q = 0; q < 8; ++q) { y0 += bitsf(pb2[q][0]); y1 += bitsf(pb2[q][2]); }
        *reinterpret_cast<f32x2*>(&sh[1024 + 2 * tid]) = f32x2{rbf(bflo(xbv) + rbf(y0)), rbf(bfhi(xbv) + rbf(y1))};
      }
    }
    __syncthreads();
    float mb[2], den[2];
#pragma unroll
    for (int bb = 0; bb < 2; ++bb) {
      f32x2 hv[5];
#pragma unroll
      for (int i = 0; i < 5; ++i) hv[i] = *reinterpret_cast<const f32x2*>(&s_h[bb][2 * (lane + 64 * i)]);
      float sum = 0.f;
#pragma unroll
      for (int i = 0; i < 5; ++i) sum += hv[i][0] + hv[i][1];
      const float mean = wave_sum(sum) * (1.0f / DM);
      float sq = 0.f;
#pragma unroll
      for (int i = 0; i < 5; ++i) { const float a = hv[i][0] - mean, e = hv[i][1] - mean; sq += a * a + e * e; }
      sq = wave_sum(sq);
      mb[bb] = rbf(mean);
      den[bb] = rbf(sqrtf(rbf(rbf(sq * (1.0f / DM)) + 1e-5f)));
    }
    float a[16];
#pragma unroll
    for (int e = 0; e < 16; ++e) a[e] = 0.f;
#pragma unroll
    for (int i = 0; i < 3; ++i) {
      const int row = 32 * wave + (lane & 31) + 256 * i;
      const int rc = row < DM ? row : 0;
      const float n0v = rbf(rbf(s_h[0][rc] - mb[0]) / den[0]);
      const float n1v = rbf(rbf(s_h[1][rc] - mb[1]) / den[1]);
      fma8(a, tl.w[i], n0v);
      fma8(a + 8, tl.w[i], n1v);
    }
    float u[8], v[4], w2r[2];
#pragma unroll
    for (int e = 0; e < 8; ++e) u[e] = red16(a[e], a[e + 8]);
#pragma unroll
    for (int e = 0; e < 4; ++e) v[e] = red8(u[e], u[e + 4]);
#pragma unroll
    for (int e = 0; e < 2; ++e) w2r[e] = red4m(v[e], v[e + 2]);
    float t = red2(w2r[0], w2r[1], lane);
    t += dpp<XOR1>(t);
    float* s_p = &s_y[0][0];
    if ((lane & 1) == 0) s_p[wave * 32 + ((lane >> 4) & 1) * 16 + (lane >> 5) * 8 + ((lane >> 3) & 1) * 4 + ((lane >> 2) & 1) * 2 + ((lane >> 1) & 1)] = t;
    __syncthreads();
    if (tid < 32) {
      float sacc = 0.f;
#pragma unroll
      for (int w = 0; w < WAVES; ++w) sacc += s_p[w * 32 + tid];
      const float hh = rbf(rbf(sacc) + bias0);
      const float rr = hh > 0.f ? hh : 0.f;
      const u32 hb = f2bf(rr * rr);
      const u32 other = __shfl_xor(hb, 16);
      const u32 pk = hb | (other << 16);
      const u32 pk1 = __shfl_xor(pk, 1);
      if (tid < 16 && (tid & 1) == 0) st_g2(rws, OFF_H3 + (u32)(192 * g + 16 * jj + tid) * 8, u32x4{pk, TAG, pk1, TAG});
    }
  } else {
    const int col0 = 80 * g + 4 * (jj - 12);
    float hres = 0.f, bias = 0.f;
    if (wave == 0) {
      const int q = lane >> 2, bb = (lane >> 1) & 1, pr = lane & 1;
      const u32 off = (lane < 32) ? OFF_H2 + (u32)((q * 2 + bb) * DM + col0 + 2 * pr) * 8 : 0x40000000u;
      const u32 xw = reinterpret_cast<const u32*>(x)[(bb * DM + col0 + 2 * pr) >> 1];
      u32x4 pv4;
      for (int spin = 0; spin < SPIN_MAX; ++spin) {
        POLL_FENCE();
        pv4 = ld_g2(rws, off);
        if (__all(lane >= 32 || tags_ok(pv4))) break;
        __builtin_amdgcn_s_sleep(2);
      }
      float y0 = 0.f, y1 = 0.f;
#pragma unroll
      for (int qq = 0; qq < 8; ++qq) {
        y0 += bitsf(__shfl(pv4[0], qq * 4 + (lane & 3)));
        y1 += bitsf(__shfl(pv4[2], qq * 4 + (lane & 3)));
      }
      const float h0 = rbf(bflo(xw) + rbf(y0)), h1 = rbf(bfhi(xw) + rbf(y1));
      const int src = ((lane >> 2) & 1) * 2 + ((lane >> 1) & 1);
      const float sh0 = __shfl(h0, src), sh1 = __shfl(h1, src);
      hres = (lane & 1) ? sh1 : sh0;
      bias = bf2f(b2[col0 + (lane & 3)]);
    }
    u64 hv[3];
    const u64* hp = reinterpret_cast<const u64*>(ws + OFF_H3) + tid;
    for (int spin = 0; spin < SPIN_MAX; ++spin) {
      bool ok = true;
#pragma unroll
      for (int i = 0; i < 3; ++i) { hv[i] = __hip_atomic_load(hp + 512 * i, __ATOMIC_RELAXED, __HIP_MEMORY_SCOPE_AGENT); ok = ok && ((u32)(hv[i] >> 32) == TAG); }
      if (__all(ok)) break;
      __builtin_amdgcn_s_sleep(2);
    }
    float y[8];
#pragma unroll
    for (int e = 0; e < 8; ++e) y[e] = 0.f;
#pragma unroll
    for (int i = 0; i < 3; ++i) {
      const float h0 = bflo((u32)hv[i]), h1 = bfhi((u32)hv[i]);
      const float w0 = bflo(tl.w2[i][0]), w1v = bfhi(tl.w2[i][0]), w2v = bflo(tl.w2[i][1]), w3 = bfhi(tl.w2[i][1]);
      y[0] = fmaf(h0, w0, y[0]); y[1] = fmaf(h0, w1v, y[1]); y[2] = fmaf(h0, w2v, y[2]); y[3] = fmaf(h0, w3, y[3]);
      y[4] = fmaf(h1, w0, y[4]); y[5] = fmaf(h1, w1v, y[5]); y[6] = fmaf(h1, w2v, y[6]); y[7] = fmaf(h1, w3, y[7]);
    }
    float u[4], v[2];
#pragma unroll
    for (int e = 0; e < 4; ++e) u[e] = red32(y[e], y[e + 4]);
#pragma unroll
    for (int e = 0; e < 2; ++e) v[e] = red16(u[e], u[e + 2]);
    float t = red8(v[0], v[1]);
    t += dpp<HMIR>(t);
    t += dpp<XOR2>(t);
    t += dpp<XOR1>(t);
    __syncthreads();
    float* s_p = &s_y[0][0];
    if (c == 0) s_p[wave * 8 + r] = t;
    __syncthreads();
    if (tid < 8) {
      float sacc = 0.f;
#pragma unroll
      for (int w = 0; w < WAVES; ++w) sacc += s_p[w * 8 + tid];
      const float o = rbf(rbf(sacc) + bias);
      out[(tid >> 2) * DM + col0 + (tid & 3)] = (u16)f2bf(hres + o);
    }
    if (bid == 255 && tid == 0) __hip_atomic_store(reinterpret_cast<u64*>(ws + OFF_EPOCH), tag64, __ATOMIC_RELAXED, __HIP_MEMORY_SCOPE_AGENT);
  }
}

extern "C" void kernel_launch(void* const* d_in, const int* in_sizes, int n_in,
                              void* d_out, int out_size, void* d_ws, size_t ws_size,
                              hipStream_t stream) {
  fused_kernel<<<256, 512, 0, stream>>>((const u16*)d_in[0], (const u16*)d_in[1], (const u16*)d_in[2], (const u16*)d_in[3],
                                        (const u16*)d_in[4], (const u16*)d_in[6], (const u16*)d_in[7], (const u16*)d_in[8],
                                        (const u16*)d_in[9], (unsigned char*)d_ws, (u16*)d_out);
}
```

```cpp
#include <hip/hip_runtime.h>
#include <hip/hip_bf16.h>
#include <stdint.h>

typedef unsigned int u32;
typedef unsigned short u16;
typedef unsigned long long u64;
using u32x4 = __attribute__((ext_vector_type(4))) unsigned;
using u32x2 = __attribute__((ext_vector_type(2))) unsigned;
using f32x2 = __attribute__((ext_vector_type(2))) float;
using bf16x2v = __attribute__((ext_vector_type(2))) __bf16;
typedef __amdgpu_buffer_rsrc_t rsrc_t;

constexpr int DM = 640, NH = 32, SK = 8192, DH = 192, FF = 1536;
constexpr int HD = NH * DH;
constexpr int NSPLIT = 4;
constexpr int KEYS_WG = SK / NSPLIT;
constexpr int WAVES = 8;
constexpr int KEYS_WAVE = KEYS_WG / WAVES;
constexpr int NBLK = KEYS_WAVE / 64;
constexpr float SCALE = 0.07216878364870322f;
constexpr float LOG2E = 1.4426950408889634f;

#define RING 16
constexpr int NW = 30;
constexpr int NLOADS = NW + NBLK * 48;

constexpr u32 OFF_EPOCH = 0;
constexpr u32 OFF_FLAGS = 1024;
constexpr u32 OFF_H1 = 4096;
constexpr u32 H1_STRIDE = 1664;
constexpr u32 OFF_H2 = OFF_H1 + 256 * H1_STRIDE;
constexpr u32 OFF_H3 = OFF_H2 + 8 * 2 * DM * 8;
constexpr u32 WS_BYTES = OFF_H3 + FF * 8;
constexpr u32 TAG = 0x5bd1e995u;
constexpr int SPIN_MAX = 1 << 18;
constexpr int AUX_NT = 2, AUX_SC1 = 16;

__device__ __forceinline__ float bf2f(u32 h) { return __builtin_bit_cast(float, h << 16); }
__device__ __forceinline__ float bflo(u32 w) { return __builtin_bit_cast(float, w << 16); }
__device__ __forceinline__ float bfhi(u32 w) { return __builtin_bit_cast(float, w & 0xffff0000u); }
__device__ __forceinline__ float rbf(float x) { return __bfloat162float(__float2bfloat16(x)); }
__device__ __forceinline__ u32 f2bf(float x) { return (u32)__builtin_bit_cast(u16, __float2bfloat16(x)); }
__device__ __forceinline__ u32 fbits(float x) { return __builtin_bit_cast(u32, x); }
__device__ __forceinline__ float bitsf(u32 x) { return __builtin_bit_cast(float, x); }

__device__ __forceinline__ float dot2(u32 k, u32 q, float acc) {
  return __builtin_amdgcn_fdot2_f32_bf16(__builtin_bit_cast(bf16x2v, k), __builtin_bit_cast(bf16x2v, q), acc, false);
}
template <int CTRL> __device__ __forceinline__ float dpp(float x) {
  return __builtin_bit_cast(float, __builtin_amdgcn_mov_dpp(__builtin_bit_cast(int, x), CTRL, 0xf, 0xf, true));
}
template <int CTRL, int BANK> __device__ __forceinline__ float dpp_upd(float old, float x) {
  return __builtin_bit_cast(float, __builtin_amdgcn_update_dpp(__builtin_bit_cast(int, old), __builtin_bit_cast(int, x), CTRL, 0xf, BANK, false));
}
constexpr int XOR1 = 0xB1, XOR2 = 0x4E, HMIR = 0x141, ROR8 = 0x128;

__device__ __forceinline__ float wave_max(float x) {
  x = fmaxf(x, dpp<XOR1>(x));
  x = fmaxf(x, dpp<XOR2>(x));
  x = fmaxf(x, dpp<HMIR>(x));
  x = fmaxf(x, dpp<ROR8>(x));
  auto s = __builtin_amdgcn_permlane16_swap(__float_as_uint(x), __float_as_uint(x), false, false);
  x = fmaxf(__uint_as_float(s[0]), __uint_as_float(s[1]));
  auto t = __builtin_amdgcn_permlane32_swap(__float_as_uint(x), __float_as_uint(x), false, false);
  return fmaxf(__uint_as_float(t[0]), __uint_as_float(t[1]));
}
__device__ __forceinline__ float wave_sum(float x) {
  x += dpp<XOR1>(x);
  x += dpp<XOR2>(x);
  x += dpp<HMIR>(x);
  x += dpp<ROR8>(x);
  auto s = __builtin_amdgcn_permlane16_swap(__float_as_uint(x), __float_as_uint(x), false, false);
  x = __uint_as_float(s[0]) + __uint_as_float(s[1]);
  auto t = __builtin_amdgcn_permlane32_swap(__float_as_uint(x), __float_as_uint(x), false, false);
  return __uint_as_float(t[0]) + __uint_as_float(t[1]);
}
__device__ __forceinline__ float red32(float a, float b) {
  auto s = __builtin_amdgcn_permlane32_swap(__float_as_uint(a), __float_as_uint(b), false, false);
  return __uint_as_float(s[0]) + __uint_as_float(s[1]);
}
__device__ __forceinline__ float red16(float a, float b) {
  auto s = __builtin_amdgcn_permlane16_swap(__float_as_uint(a), __float_as_uint(b), false, false);
  return __uint_as_float(s[0]) + __uint_as_float(s[1]);
}
__device__ __forceinline__ float red8(float a, float b) { return dpp_upd<ROR8, 0xC>(a, b) + dpp_upd<ROR8, 0x3>(b, a); }
__device__ __forceinline__ float red4m(float a, float b) { return dpp_upd<HMIR, 0xA>(a, b) + dpp_upd<HMIR, 0x5>(b, a); }
__device__ __forceinline__ float red2(float a, float b, int lane) { const float t = a + dpp<XOR2>(a), u = b + dpp<XOR2>(b); return (lane & 2) ? u : t; }
__device__ __forceinline__ float red1(float a, float b, int lane) { const float t = a + dpp<XOR1>(a), u = b + dpp<XOR1>(b); return (lane & 1) ? u : t; }

__device__ __forceinline__ void tr_reduce24(float (&a)[24], float (&o)[3]) {
  float b[12], c[6];
#pragma unroll
  for (int v = 0; v < 12; ++v) b[v] = red32(a[v], a[v + 12]);
#pragma unroll
  for (int v = 0; v < 6; ++v) c[v] = red16(b[v], b[v + 6]);
#pragma unroll
  for (int v = 0; v < 3; ++v) o[v] = red8(c[v], c[v + 3]);
}

struct Streams { rsrc_t rw, rk, rv; int vo_w, vo_kv; };

template <int N> __device__ __forceinline__ u32x4 issue(const Streams& s) {
  if constexpr (N < NW) {
    constexpr int t = N / 3, j = N % 3;
    return __builtin_amdgcn_raw_buffer_load_b128(s.rw, s.vo_w, t * 8 * HD * 2 + j * 128, AUX_NT);
  } else {
    constexpr int n = N - NW, blk = n / 48, rem = n % 48, isv = rem / 24, q = rem % 24, i = q / 3, j = q % 3;
    constexpr int off = (blk * 64 + i * 8) * DH * 2 + j * 128;
    if constexpr (isv) return __builtin_amdgcn_raw_buffer_load_b128(s.rv, s.vo_kv, off, AUX_NT);
    else return __builtin_amdgcn_raw_buffer_load_b128(s.rk, s.vo_kv, off, AUX_NT);
  }
}
struct Tail {
  rsrc_t rwo, rw1, rw2; int vo_wo, vo_1, vo_2; bool role1;
  const u64* fp; u64 flag;
  u32x2 wo[12]; u32x4 w[3]; u32x2 w2[3];
};
__device__ __forceinline__ void issue_tail(Tail& t) {
#pragma unroll
  for (int i = 0; i < 12; ++i) t.wo[i] = __builtin_amdgcn_raw_buffer_load_b64(t.rwo, t.vo_wo, i * 8 * DM * 2, AUX_NT);
#pragma unroll
  for (int i = 0; i < 3; ++i) t.w[i] = __builtin_amdgcn_raw_buffer_load_b128(t.rw1, t.vo_1, i * 256 * FF * 2, AUX_NT);
#pragma unroll
  for (int i = 0; i < 3; ++i) t.w2[i] = __builtin_amdgcn_raw_buffer_load_b64(t.rw2, t.vo_2, i * 512 * DM * 2, AUX_NT);
}
template <int N> __device__ __forceinline__ void refill(u32x4 (&ring)[RING], const Streams& s, Tail& t) {
  if constexpr (N + RING < NLOADS) ring[N % RING] = issue<N + RING>(s);
  else if constexpr (N + RING == NLOADS) issue_tail(t);
  if constexpr (N == NW + 2 * 48) t.flag = __hip_atomic_load(t.fp, __ATOMIC_RELAXED, __HIP_MEMORY_SCOPE_AGENT);
  __builtin_amdgcn_sched_barrier(0);
}
template <int N, int E> __device__ __forceinline__ void prologue(u32x4 (&ring)[RING], const Streams& s) {
  if constexpr (N < E) { ring[N] = issue<N>(s); prologue<N + 1, E>(ring, s); }
}
__device__ __forceinline__ void fma8(float* acc, const u32x4 w, const float p) {
#pragma unroll
  for (int k = 0; k < 4; ++k) {
    acc[2 * k] = fmaf(p, bflo(w[k]), acc[2 * k]);
    acc[2 * k + 1] = fmaf(p, bfhi(w[k]), acc[2 * k + 1]);
  }
}
template <int T> __device__ __forceinline__ void qproj(u32x4 (&ring)[RING], const Streams& s, Tail& tl, float (&acc)[24], const float (&xn)[10]) {
  if constexpr (T < 10) {
    fma8(acc + 0, ring[(T * 3 + 0) % RING], xn[T]);  refill<T * 3 + 0>(ring, s, tl);
    fma8(acc + 8, ring[(T * 3 + 1) % RING], xn[T]);  refill<T * 3 + 1>(ring, s, tl);
    fma8(acc + 16, ring[(T * 3 + 2) % RING], xn[T]); refill<T * 3 + 2>(ring, s, tl);
    qproj<T + 1>(ring, s, tl, acc, xn);
  }
}
template <int BASE, int I> __device__ __forceinline__ void qk(u32x4 (&ring)[RING], const Streams& s, Tail& tl, float (&xs)[8], const u32x4 (&qf)[3]) {
  if constexpr (I < 8) {
    float d = 0.f;
#pragma unroll
    for (int k = 0; k < 4; ++k) d = dot2(ring[(BASE + I * 3 + 0) % RING][k], qf[0][k], d);
    refill<BASE + I * 3 + 0>(ring, s, tl);
#pragma unroll
    for (int k = 0; k < 4; ++k) d = dot2(ring[(BASE + I * 3 + 1) % RING][k], qf[1][k], d);
    refill<BASE + I * 3 + 1>(ring, s, tl);
#pragma unroll
    for (int k = 0; k < 4; ++k) d = dot2(ring[(BASE + I * 3 + 2) % RING][k], qf[2][k], d);
    refill<BASE + I * 3 + 2>(ring, s, tl);
    xs[I] = d;
    qk<BASE, I + 1>(ring, s, tl, xs, qf);
  }
}
template <int BASE, int I> __device__ __forceinline__ void pv(u32x4 (&ring)[RING], const Streams& s, Tail& tl, float (&acc)[24], const float (&pb)[8]) {
  if constexpr (I < 8) {
    fma8(acc + 0, ring[(BASE + I * 3 + 0) % RING], pb[I]);  refill<BASE + I * 3 + 0>(ring, s, tl);
    fma8(acc + 8, ring[(BASE + I * 3 + 1) % RING], pb[I]);  refill<BASE + I * 3 + 1>(ring, s, tl);
    fma8(acc + 16, ring[(BASE + I * 3 + 2) % RING], pb[I]); refill<BASE + I * 3 + 2>(ring, s, tl);
    pv<BASE, I + 1>(ring, s, tl, acc, pb);
  }
}
template <int BLK> __device__ __forceinline__ void blocks(u32x4 (&ring)[RING], const Streams& s, Tail& tl, float (&acc)[24], const u32x4 (&qf)[3],
                                                          float& m, float& l, const int c) {
  if constexpr (BLK < NBLK) {
    constexpr int BASE = NW + BLK * 48;
    float xsx[8];
    qk<BASE, 0>(ring, s, tl, xsx, qf);
    float y[4], z[2];
#pragma unroll
    for (int i = 0; i < 4; ++i) y[i] = red4m(xsx[i], xsx[i + 4]);
#pragma unroll
    for (int i = 0; i < 2; ++i) z[i] = red2(y[i], y[i + 2], c);
    float sc = red1(z[0], z[1], c);
    sc = rbf(rbf(sc) * SCALE);
    const float bm = wave_max(sc);
    const float mn = fmaxf(m, bm);
    const float alpha = __builtin_amdgcn_exp2f((m - mn) * LOG2E);
    const float p = __builtin_amdgcn_exp2f((sc - mn) * LOG2E);
    l = fmaf(l, alpha, p);
    m = mn;
    if constexpr (BLK > 0) {
#pragma unroll
      for (int n = 0; n < 24; ++n) acc[n] *= alpha;
    }
    float pb[8];
    {
      const float a0 = dpp<0x00>(p), a1 = dpp<0x55>(p), a2 = dpp<0xAA>(p), a3 = dpp<0xFF>(p);
      pb[0] = dpp_upd<HMIR, 0xA>(a0, a0); pb[4] = dpp_upd<HMIR, 0x5>(a0, a0);
      pb[1] = dpp_upd<HMIR, 0xA>(a1, a1); pb[5] = dpp_upd<HMIR, 0x5>(a1, a1);
      pb[2] = dpp_upd<HMIR, 0xA>(a2, a2); pb[6] = dpp_upd<HMIR, 0x5>(a2, a2);
      pb[3] = dpp_upd<HMIR, 0xA>(a3, a3); pb[7] = dpp_upd<HMIR, 0x5>(a3, a3);
    }
    pv<BASE + 24, 0>(ring, s, tl, acc, pb);
    blocks<BLK + 1>(ring, s, tl, acc, qf, m, l, c);
  }
}

__device__ __forceinline__ u32x4 ld_g2(rsrc_t r, u32 off) { return __builtin_amdgcn_raw_buffer_load_b128(r, off, 0, AUX_SC1); }
__device__ __forceinline__ void st_g2(rsrc_t r, u32 off, u32x4 v) { __builtin_amdgcn_raw_buffer_store_b128(v, r, off, 0, AUX_SC1); }
__device__ __forceinline__ bool tags_ok(const u32x4 v) { return v[1] == TAG && v[3] == TAG; }
#define POLL_FENCE() asm volatile("" ::: "memory")
#define STAMP(i) do { } while (0)

__global__ void __launch_bounds__(512, 2)
fused_kernel(const u16* __restrict__ x, const u16* __restrict__ kc, const u16* __restrict__ vc,
             const u16* __restrict__ wq, const u16* __restrict__ wo, const u16* __restrict__ w1, const u16* __restrict__ b1,
             const u16* __restrict__ w2, const u16* __restrict__ b2, unsigned char* __restrict__ ws, u16* __restrict__ out) {
  __shared__ __attribute__((aligned(16))) float s_red[WAVES][DH];
  __shared__ __attribute__((aligned(16))) float s_h[2][DM];
  __shared__ __attribute__((aligned(16))) float s_y[WAVES][40];
  __shared__ __attribute__((aligned(16))) u16 s_q[DH];
  __shared__ float s_m[WAVES], s_l[WAVES];

  const int bid = blockIdx.x;
  const int g = bid & 7, jj = bid >> 3;
  const int h = g * 4 + (jj >> 3);
  const int b = (jj >> 2) & 1, sp = jj & 3;
  const int tid = threadIdx.x, lane = tid & 63;
  const int wave = __builtin_amdgcn_readfirstlane(tid >> 6);
  const int r = lane >> 3, c = lane & 7;
  const bool role1 = jj < 12;

  STAMP(0);
  const rsrc_t rws = __builtin_amdgcn_make_buffer_rsrc((void*)ws, 0, WS_BYTES, 0x00020000);
  const u32 my_h1 = OFF_H1 + (u32)(g * 32 + jj) * H1_STRIDE;
  if (wave == 7) {
    const u32x4 z4 = {0u, 0u, 0u, 0u};
    st_g2(rws, my_h1 + lane * 16, z4);
    if (lane < 40) st_g2(rws, my_h1 + 1024 + lane * 16, z4);
    if (lane < 20) st_g2(rws, OFF_H2 + (u32)((g * 2 + lane / 10) * DM + 20 * jj + 2 * (lane % 10)) * 8, z4);
    if (role1 && lane < 8) st_g2(rws, OFF_H3 + (u32)(192 * g + 16 * jj + 2 * lane) * 8, z4);
  }
  const u16* xb = x + b * DM;
  u32 xs[5];
#pragma unroll
  for (int i = 0; i < 5; ++i) xs[i] = reinterpret_cast<const u32*>(xb)[lane + 64 * i];
  u32 xr[10];
#pragma unroll
  for (int t = 0; t < 10; ++t) xr[t] = xb[80 * wave + 8 * t + r];

  Streams s;
  Tail tl;
  {
    const u16* wqp = wq + (size_t)(80 * wave) * HD + h * DH;
    const size_t kvoff = ((size_t)(b * NH + h) * SK + sp * KEYS_WG + wave * KEYS_WAVE) * DH;
    s.rw = __builtin_amdgcn_make_buffer_rsrc((void*)wqp, 0, (79 * HD + DH) * 2, 0x00020000);
    s.rk = __builtin_amdgcn_make_buffer_rsrc((void*)(kc + kvoff), 0, KEYS_WAVE * DH * 2, 0x00020000);
    s.rv = __builtin_amdgcn_make_buffer_rsrc((void*)(vc + kvoff), 0, KEYS_WAVE * DH * 2, 0x00020000);
    s.vo_w = r * HD * 2 + c * 16;
    s.vo_kv = r * DH * 2 + c * 16;
    tl.rwo = __builtin_amdgcn_make_buffer_rsrc((void*)(wo + (size_t)(768 * g + 96 * wave) * DM), 0, 96 * DM * 2, 0x00020000);
    tl.vo_wo = (c < 5) ? (r * DM + 20 * jj + 4 * c) * 2 : 0x40000000;
    tl.role1 = role1;
    tl.fp = reinterpret_cast<const u64*>(ws + OFF_FLAGS) + (tid & 255);
    tl.rw1 = __builtin_amdgcn_make_buffer_rsrc((void*)w1, 0, DM * FF * 2, 0x00020000);
    tl.vo_1 = role1 ? ((32 * wave + (lane & 31)) * FF + 192 * g + 16 * jj + 8 * (lane >> 5)) * 2 : 0x40000000;
    tl.rw2 = __builtin_amdgcn_make_buffer_rsrc((void*)w2, 0, FF * DM * 2, 0x00020000);
    tl.vo_2 = role1 ? 0x40000000 : (tid * DM + 80 * g + 4 * (jj - 12)) * 2;
  }
  u32x4 ring[RING];
  __builtin_amdgcn_sched_barrier(0);
  prologue<0, RING>(ring, s);
  const u64 epoch = __hip_atomic_load(reinterpret_cast<u64*>(ws + OFF_EPOCH), __ATOMIC_RELAXED, __HIP_MEMORY_SCOPE_AGENT);
  __builtin_amdgcn_sched_barrier(0);

  float xn[10];
  {
    float sum = 0.f;
#pragma unroll
    for (int i = 0; i < 5; ++i) sum += bflo(xs[i]) + bfhi(xs[i]);
    sum = wave_sum(sum);
    const float mean = sum * (1.0f / DM);
    float sq = 0.f;
#pragma unroll
    for (int i = 0; i < 5; ++i) { float a = bflo(xs[i]) - mean, bb = bfhi(xs[i]) - mean; sq += a * a + bb * bb; }
    sq = wave_sum(sq);
    const float mb = rbf(mean), vb = rbf(sq * (1.0f / DM));
    const float den = rbf(sqrtf(rbf(vb + 1e-5f)));
#pragma unroll
    for (int t = 0; t < 10; ++t) xn[t] = rbf(rbf(bf2f(xr[t]) - mb) / den);
  }
  float acc[24];
#pragma unroll
  for (int n = 0; n < 24; ++n) acc[n] = 0.f;
  qproj<0>(ring, s, tl, acc, xn);

  const u64 tag64 = epoch + 1ull;
  asm volatile("s_waitcnt vmcnt(%0)" ::"n"(RING) : "memory");
  __builtin_amdgcn_raw_buffer_store_b64(u32x2{(u32)tag64, (u32)(tag64 >> 32)}, rws, (tid == 448) ? OFF_FLAGS + bid * 8 : 0x40000000u, 0, AUX_SC1);
  __builtin_amdgcn_sched_barrier(0);

  const int n0 = 12 * (lane >> 5) + 6 * ((lane >> 4) & 1) + 3 * ((lane >> 3) & 1);
  {
    float o3[3];
    tr_reduce24(acc, o3);
#pragma unroll
    for (int v = 0; v < 3; ++v) { const int n = n0 + v; s_red[wave][(n >> 3) * 64 + c * 8 + (n & 7)] = o3[v]; }
  }
  __syncthreads();
  if (tid < DH) {
    float q = 0.f;
#pragma unroll
    for (int w = 0; w < WAVES; ++w) q += s_red[w][tid];
    s_q[tid] = (u16)f2bf(q);
  }
  __syncthreads();
  u32x4 qf[3];
#pragma unroll
  for (int j = 0; j < 3; ++j) qf[j] = *reinterpret_cast<const u32x4*>(&s_q[j * 64 + c * 8]);

  STAMP(1);
  float m = -INFINITY, l = 0.f;
#pragma unroll
  for (int n = 0; n < 24; ++n) acc[n] = 0.f;
  blocks<0>(ring, s, tl, acc, qf, m, l, c);

  STAMP(2);
  l = wave_sum(l);
  {
    float o3[3];
    tr_reduce24(acc, o3);
#pragma unroll
    for (int v = 0; v < 3; ++v) { const int n = n0 + v; s_red[wave][(n >> 3) * 64 + c * 8 + (n & 7)] = o3[v]; }
    if (lane == 0) { s_m[wave] = m; s_l[wave] = l; }
  }
  if (!__all(tl.flag == tag64)) {
    for (int spin = 0; spin < SPIN_MAX; ++spin) {
      const u64 f = __hip_atomic_load(tl.fp, __ATOMIC_RELAXED, __HIP_MEMORY_SCOPE_AGENT);
      if (__all(f == tag64)) break;
      __builtin_amdgcn_s_sleep(4);
    }
  }
  __syncthreads();
  if (tid <= 96) {
    float M = s_m[0];
#pragma unroll
    for (int w = 1; w < WAVES; ++w) M = fmaxf(M, s_m[w]);
    float L = 0.f, o0 = 0.f, o1 = 0.f;
    const int d = (tid < 96) ? 2 * tid : 0;
#pragma unroll
    for (int w = 0; w < WAVES; ++w) {
      const float e = __builtin_amdgcn_exp2f((s_m[w] - M) * LOG2E);
      const f32x2 a = *reinterpret_cast<const f32x2*>(&s_red[w][d]);
      L = fmaf(e, s_l[w], L);
      o0 = fmaf(e, a[0], o0);
      o1 = fmaf(e, a[1], o1);
    }
    if (tid == 96) { o0 = M; o1 = L; }
    st_g2(rws, my_h1 + tid * 16, u32x4{fbits(o0), TAG, fbits(o1), TAG});
  }
  __syncthreads();
  STAMP(3);

  float* s_o = &s_red[0][0];
  {
    u32x4 pa[4], pb2[4];
    const u32 base = OFF_H1 + (u32)(g * 32 + wave * 4) * H1_STRIDE;
    const u32 oa = lane * 16, ob = (lane < 33) ? 1024 + lane * 16 : 0x40000000u;
    unsigned done = 0;
    for (int spin = 0; spin < SPIN_MAX; ++spin) {
      POLL_FENCE();
#pragma unroll
      for (int q = 0; q < 4; ++q) {
        if (!(done & (1u << q))) {
          pa[q] = ld_g2(rws, base + q * H1_STRIDE + oa);
          pb2[q] = ld_g2(rws, (lane < 33) ? base + q * H1_STRIDE + ob : ob);
        }
      }
#pragma unroll
      for (int q = 0; q < 4; ++q) {
        const bool ok = tags_ok(pa[q]) && (lane >= 33 || tags_ok(pb2[q]));
        if (__all(ok)) done |= 1u << q;
      }
      if (done == 15u) break;
      __builtin_amdgcn_s_sleep(2);
    }
    float ms[4], ls[4];
#pragma unroll
    for (int q = 0; q < 4; ++q) {
      ms[q] = bitsf(__builtin_amdgcn_readlane(pb2[q][0], 32));
      ls[q] = bitsf(__builtin_amdgcn_readlane(pb2[q][2], 32));
    }
    const float M = fmaxf(fmaxf(ms[0], ms[1]), fmaxf(ms[2], ms[3]));
    float L = 0.f, a0 = 0.f, a1 = 0.f, b0 = 0.f, b1v = 0.f;
#pragma unroll
    for (int q = 0; q < 4; ++q) {
      const float e = __builtin_amdgcn_exp2f((ms[q] - M) * LOG2E);
      L = fmaf(e, ls[q], L);
      a0 = fmaf(e, bitsf(pa[q][0]), a0); a1 = fmaf(e, bitsf(pa[q][2]), a1);
      b0 = fmaf(e, bitsf(pb2[q][0]), b0); b1v = fmaf(e, bitsf(pb2[q][2]), b1v);
    }
    const int rowb = (wave >> 1) * DH, bb = wave & 1;
    s_o[(rowb + 2 * lane) * 2 + bb] = rbf(a0 / L);
    s_o[(rowb + 2 * lane + 1) * 2 + bb] = rbf(a1 / L);
    if (lane < 32) {
      s_o[(rowb + 128 + 2 * lane) * 2 + bb] = rbf(b0 / L);
      s_o[(rowb + 128 + 2 * lane + 1) * 2 + bb] = rbf(b1v / L);
    }
  }
  __syncthreads();
  STAMP(4);
  {
    float y[8];
#pragma unroll
    for (int e = 0; e < 8; ++e) y[e] = 0.f;
#pragma unroll
    for (int i = 0; i < 12; ++i) {
      const f32x2 o = *reinterpret_cast<const f32x2*>(&s_o[(96 * wave + 8 * i + r) * 2]);
      const float w0 = bflo(tl.wo[i][0]), w1v = bfhi(tl.wo[i][0]), w2v = bflo(tl.wo[i][1]), w3 = bfhi(tl.wo[i][1]);
      y[0] = fmaf(o[0], w0, y[0]); y[1] = fmaf(o[0], w1v, y[1]); y[2] = fmaf(o[0], w2v, y[2]); y[3] = fmaf(o[0], w3, y[3]);
      y[4] = fmaf(o[1], w0, y[4]); y[5] = fmaf(o[1], w1v, y[5]); y[6] = fmaf(o[1], w2v, y[6]); y[7] = fmaf(o[1], w3, y[7]);
    }
    float u[4], v[2];
#pragma unroll
    for (int e = 0; e < 4; ++e) u[e] = red32(y[e], y[e + 4]);
#pragma unroll
    for (int e = 0; e < 2; ++e) v[e] = red16(u[e], u[e + 2]);
    const float t = red8(v[0], v[1]);
    if (c < 5) s_y[wave][(lane >> 5) * 20 + 4 * c + 2 * ((lane >> 4) & 1) + ((lane >> 3) & 1)] = t;
  }
  __syncthreads();
  if (tid < 20) {
    float y0 = 0.f, y1 = 0.f;
#pragma unroll
    for (int w = 0; w < WAVES; ++w) { const f32x2 a = *reinterpret_cast<const f32x2*>(&s_y[w][2 * tid]); y0 += a[0]; y1 += a[1]; }
    st_g2(rws, OFF_H2 + (u32)((g * 2 + tid / 10) * DM + 20 * jj + 2 * (tid % 10)) * 8, u32x4{fbits(y0), TAG, fbits(y1), TAG});
  }

  STAMP(5);
  if (role1) {
    const u32 xa = reinterpret_cast<const u32*>(x)[tid];
    const u32 xbv = (tid < 128) ? reinterpret_cast<const u32*>(x)[512 + tid] : 0u;
    const float bias0 = bf2f(b1[192 * g + 16 * jj + (tid & 15)]);
    {
      u32x4 pa[8], pb2[8];
      const u32 oa = OFF_H2 + tid * 16, ob = (tid < 128) ? OFF_H2 + (512 + tid) * 16 : 0x40000000u;
      for (int spin = 0; spin < SPIN_MAX; ++spin) {
        POLL_FENCE();
#pragma unroll
        for (int q = 0; q < 8; ++q) pa[q] = ld_g2(rws, oa + q * (2 * DM * 8));
        if (wave < 2) {
#pragma unroll
          for (int q = 0; q < 8; ++q) pb2[q] = ld_g2(rws, ob + q * (2 * DM * 8));
        }
        bool ok = true;
#pragma unroll
        for (int q = 0; q < 8; ++q) ok = ok && tags_ok(pa[q]);
        if (wave < 2) {
#pragma unroll
          for (int q = 0; q < 8; ++q) ok = ok && tags_ok(pb2[q]);
        }
        if (__all(ok)) break;
        __builtin_amdgcn_s_sleep(2);
      }
      float y0 = 0.f, y1 = 0.f;
#pragma unroll
      for (int q = 0; q < 8; ++q) { y0 += bitsf(pa[q][0]); y1 += bitsf(pa[q][2]); }
      float* sh = &s_h[0][0];
      *reinterpret_cast<f32x2*>(&sh[2 * tid]) = f32x2{rbf(bflo(xa) + rbf(y0)), rbf(bfhi(xa) + rbf(y1))};
      if (wave < 2) {
        y0 = 0.f; y1 = 0.f;
#pragma unroll
        for (int q = 0; q < 8; ++q) { y0 += bitsf(pb2[q][0]); y1 += bitsf(pb2[q][2]); }
        *reinterpret_cast<f32x2*>(&sh[1024 + 2 * tid]) = f32x2{rbf(bflo(xbv) + rbf(y0)), rbf(bfhi(xbv) + rbf(y1))};
      }
    }
    __syncthreads();
    STAMP(6);
    float mb[2], den[2];
#pragma unroll
    for (int bb = 0; bb < 2; ++bb) {
      f32x2 hv[5];
#pragma unroll
      for (int i = 0; i < 5; ++i) hv[i] = *reinterpret_cast<const f32x2*>(&s_h[bb][2 * (lane + 64 * i)]);
      float sum = 0.f;
#pragma unroll
      for (int i = 0; i < 5; ++i) sum += hv[i][0] + hv[i][1];
      const float mean = wave_sum(sum) * (1.0f / DM);
      float sq = 0.f;
#pragma unroll
      for (int i = 0; i < 5; ++i) { const float a = hv[i][0] - mean, e = hv[i][1] - mean; sq += a * a + e * e; }
      sq = wave_sum(sq);
      mb[bb] = rbf(mean);
      den[bb] = rbf(sqrtf(rbf(rbf(sq * (1.0f / DM)) + 1e-5f)));
    }
    float a[16];
#pragma unroll
    for (int e = 0; e < 16; ++e) a[e] = 0.f;
#pragma unroll
    for (int i = 0; i < 3; ++i) {
      const int row = 32 * wave + (lane & 31) + 256 * i;
      const int rc = row < DM ? row : 0;
      const float n0v = rbf(rbf(s_h[0][rc] - mb[0]) / den[0]);
      const float n1v = rbf(rbf(s_h[1][rc] - mb[1]) / den[1]);
      fma8(a, tl.w[i], n0v);
      fma8(a + 8, tl.w[i], n1v);
    }
    float u[8], v[4], w2r[2];
#pragma unroll
    for (int e = 0; e < 8; ++e) u[e] = red16(a[e], a[e + 8]);
#pragma unroll
    for (int e = 0; e < 4; ++e) v[e] = red8(u[e], u[e + 4]);
#pragma unroll
    for (int e = 0; e < 2; ++e) w2r[e] = red4m(v[e], v[e + 2]);
    float t = red2(w2r[0], w2r[1], lane);
    t += dpp<XOR1>(t);
    float* s_p = &s_y[0][0];
    if ((lane & 1) == 0) s_p[wave * 32 + ((lane >> 4) & 1) * 16 + (lane >> 5) * 8 + ((lane >> 3) & 1) * 4 + ((lane >> 2) & 1) * 2 + ((lane >> 1) & 1)] = t;
    __syncthreads();
    if (tid < 32) {
      float sacc = 0.f;
#pragma unroll
      for (int w = 0; w < WAVES; ++w) sacc += s_p[w * 32 + tid];
      const float hh = rbf(rbf(sacc) + bias0);
      const float rr = hh > 0.f ? hh : 0.f;
      const u32 hb = f2bf(rr * rr);
      const u32 other = __shfl_xor(hb, 16);
      const u32 pk = hb | (other << 16);
      const u32 pk1 = __shfl_xor(pk, 1);
      if (tid < 16 && (tid & 1) == 0) st_g2(rws, OFF_H3 + (u32)(192 * g + 16 * jj + tid) * 8, u32x4{pk, TAG, pk1, TAG});
    }
    STAMP(7);
  } else {
    const int col0 = 80 * g + 4 * (jj - 12);
    float hres = 0.f, bias = 0.f;
    if (wave == 0) {
      const int q = lane >> 2, bb = (lane >> 1) & 1, pr = lane & 1;
      const u32 off = (lane < 32) ? OFF_H2 + (u32)((q * 2 + bb) * DM + col0 + 2 * pr) * 8 : 0x40000000u;
      const u32 xw = reinterpret_cast<const u32*>(x)[(bb * DM + col0 + 2 * pr) >> 1];
      u32x4 pv4;
      for (int spin = 0; spin < SPIN_MAX; ++spin) {
        POLL_FENCE();
        pv4 = ld_g2(rws, off);
        if (__all(lane >= 32 || tags_ok(pv4))) break;
        __builtin_amdgcn_s_sleep(2);
      }
      float y0 = 0.f, y1 = 0.f;
#pragma unroll
      for (int qq = 0; qq < 8; ++qq) {
        y0 += bitsf(__shfl(pv4[0], qq * 4 + (lane & 3)));
        y1 += bitsf(__shfl(pv4[2], qq * 4 + (lane & 3)));
      }
      const float h0 = rbf(bflo(xw) + rbf(y0)), h1 = rbf(bfhi(xw) + rbf(y1));
      const int src = ((lane >> 2) & 1) * 2 + ((lane >> 1) & 1);
      const float sh0 = __shfl(h0, src), sh1 = __shfl(h1, src);
      hres = (lane & 1) ? sh1 : sh0;
      bias = bf2f(b2[col0 + (lane & 3)]);
    }
    STAMP(6);
    u64 hv[3];
    const u64* hp = reinterpret_cast<const u64*>(ws + OFF_H3) + tid;
    for (int spin = 0; spin < SPIN_MAX; ++spin) {
      bool ok = true;
#pragma unroll
      for (int i = 0; i < 3; ++i) { hv[i] = __hip_atomic_load(hp + 512 * i, __ATOMIC_RELAXED, __HIP_MEMORY_SCOPE_AGENT); ok = ok && ((u32)(hv[i] >> 32) == TAG); }
      if (__all(ok)) break;
      __builtin_amdgcn_s_sleep(2);
    }
    STAMP(7);
    float y[8];
#pragma unroll
    for (int e = 0; e < 8; ++e) y[e] = 0.f;
#pragma unroll
    for (int i = 0; i < 3; ++i) {
      const float h0 = bflo((u32)hv[i]), h1 = bfhi((u32)hv[i]);
      const float w0 = bflo(tl.w2[i][0]), w1v = bfhi(tl.w2[i][0]), w2v = bflo(tl.w2[i][1]), w3 = bfhi(tl.w2[i][1]);
      y[0] = fmaf(h0, w0, y[0]); y[1] = fmaf(h0, w1v, y[1]); y[2] = fmaf(h0, w2v, y[2]); y[3] = fmaf(h0, w3, y[3]);
      y[4] = fmaf(h1, w0, y[4]); y[5] = fmaf(h1, w1v, y[5]); y[6] = fmaf(h1, w2v, y[6]); y[7] = fmaf(h1, w3, y[7]);
    }
    float u[4], v[2];
#pragma unroll
    for (int e = 0; e < 4; ++e) u[e] = red32(y[e], y[e + 4]);
#pragma unroll
    for (int e = 0; e < 2; ++e) v[e] = red16(u[e], u[e + 2]);
    float t = red8(v[0], v[1]);
    t += dpp<HMIR>(t);
    t += dpp<XOR2>(t);
    t += dpp<XOR1>(t);
    __syncthreads();
    float* s_p = &s_y[0][0];
    if (c == 0) s_p[wave * 8 + r] = t;
    __syncthreads();
    if (tid < 8) {
      float sacc = 0.f;
#pragma unroll
      for (int w = 0; w < WAVES; ++w) sacc += s_p[w * 8 + tid];
      const float o = rbf(rbf(sacc) + bias);
      out[(tid >> 2) * DM + col0 + (tid & 3)] = (u16)f2bf(hres + o);
    }
    STAMP(8);
    if (bid == 255 && tid == 0) __hip_atomic_store(reinterpret_cast<u64*>(ws + OFF_EPOCH), tag64, __ATOMIC_RELAXED, __HIP_MEMORY_SCOPE_AGENT);
  }
}

extern "C" void kernel_launch(void* const* d_in, const int* in_sizes, int n_in,
                              void* d_out, int out_size, void* d_ws, size_t ws_size,
                              hipStream_t stream) {
  fused_kernel<<<256, 512, 0, stream>>>((const u16*)d_in[0], (const u16*)d_in[1], (const u16*)d_in[2], (const u16*)d_in[3],
                                        (const u16*)d_in[4], (const u16*)d_in[6], (const u16*)d_in[7], (const u16*)d_in[8],
                                        (const u16*)d_in[9], (unsigned char*)d_ws, (u16*)d_out);
}
```

```cpp
#include <hip/hip_runtime.h>
#include <hip/hip_bf16.h>
#include <stdint.h>

typedef unsigned int u32;
typedef unsigned short u16;
typedef unsigned long long u64;
using u32x4 = __attribute__((ext_vector_type(4))) unsigned;
using u32x2 = __attribute__((ext_vector_type(2))) unsigned;
using f32x2 = __attribute__((ext_vector_type(2))) float;
using bf16x2v = __attribute__((ext_vector_type(2))) __bf16;
typedef __amdgpu_buffer_rsrc_t rsrc_t;

constexpr int DM = 640, NH = 32, SK = 8192, DH = 192, FF = 1536;
constexpr int HD = NH * DH;
constexpr int NSPLIT = 4;
constexpr int KEYS_WG = SK / NSPLIT;
constexpr int WAVES = 8;
constexpr int KEYS_WAVE = KEYS_WG / WAVES;
constexpr int NBLK = KEYS_WAVE / 64;
constexpr float SCALE = 0.07216878364870322f;
constexpr float LOG2E = 1.4426950408889634f;

#define RING 16
constexpr int NW = 30;
constexpr int NLOADS = NW + NBLK * 48;

constexpr u32 OFF_EPOCH = 0;
constexpr u32 OFF_FLAGS = 1024;
constexpr u32 OFF_H1 = 4096;
constexpr u32 H1_STRIDE = 1664;
constexpr u32 OFF_H2 = OFF_H1 + 256 * H1_STRIDE;
constexpr u32 OFF_H3 = OFF_H2 + 8 * 2 * DM * 8;
constexpr u32 WS_BYTES = OFF_H3 + FF * 8;
constexpr u32 TAG = 0x5bd1e995u;
constexpr int SPIN_MAX = 1 << 18;
constexpr int AUX_NT = 2, AUX_SC1 = 16;

__device__ __forceinline__ float bf2f(u32 h) { return __builtin_bit_cast(float, h << 16); }
__device__ __forceinline__ float bflo(u32 w) { return __builtin_bit_cast(float, w << 16); }
__device__ __forceinline__ float bfhi(u32 w) { return __builtin_bit_cast(float, w & 0xffff0000u); }
__device__ __forceinline__ float rbf(float x) { return __bfloat162float(__float2bfloat16(x)); }
__device__ __forceinline__ u32 f2bf(float x) { return (u32)__builtin_bit_cast(u16, __float2bfloat16(x)); }
__device__ __forceinline__ u32 fbits(float x) { return __builtin_bit_cast(u32, x); }
__device__ __forceinline__ float bitsf(u32 x) { return __builtin_bit_cast(float, x); }

__device__ __forceinline__ float dot2(u32 k, u32 q, float acc) {
  return __builtin_amdgcn_fdot2_f32_bf16(__builtin_bit_cast(bf16x2v, k), __builtin_bit_cast(bf16x2v, q), acc, false);
}
template <int CTRL> __device__ __forceinline__ float dpp(float x) {
  return __builtin_bit_cast(float, __builtin_amdgcn_mov_dpp(__builtin_bit_cast(int, x), CTRL, 0xf, 0xf, true));
}
template <int CTRL, int BANK> __device__ __forceinline__ float dpp_upd(float old, float x) {
  return __builtin_bit_cast(float, __builtin_amdgcn_update_dpp(__builtin_bit_cast(int, old), __builtin_bit_cast(int, x), CTRL, 0xf, BANK, false));
}
constexpr int XOR1 = 0xB1, XOR2 = 0x4E, HMIR = 0x141, ROR8 = 0x128;

__device__ __forceinline__ float wave_max(float x) {
  x = fmaxf(x, dpp<XOR1>(x));
  x = fmaxf(x, dpp<XOR2>(x));
  x = fmaxf(x, dpp<HMIR>(x));
  x = fmaxf(x, dpp<ROR8>(x));
  auto s = __builtin_amdgcn_permlane16_swap(__float_as_uint(x), __float_as_uint(x), false, false);
  x = fmaxf(__uint_as_float(s[0]), __uint_as_float(s[1]));
  auto t = __builtin_amdgcn_permlane32_swap(__float_as_uint(x), __float_as_uint(x), false, false);
  return fmaxf(__uint_as_float(t[0]), __uint_as_float(t[1]));
}
__device__ __forceinline__ float wave_sum(float x) {
  x += dpp<XOR1>(x);
  x += dpp<XOR2>(x);
  x += dpp<HMIR>(x);
  x += dpp<ROR8>(x);
  auto s = __builtin_amdgcn_permlane16_swap(__float_as_uint(x), __float_as_uint(x), false, false);
  x = __uint_as_float(s[0]) + __uint_as_float(s[1]);
  auto t = __builtin_amdgcn_permlane32_swap(__float_as_uint(x), __float_as_uint(x), false, false);
  return __uint_as_float(t[0]) + __uint_as_float(t[1]);
}
__device__ __forceinline__ float red32(float a, float b) {
  auto s = __builtin_amdgcn_permlane32_swap(__float_as_uint(a), __float_as_uint(b), false, false);
  return __uint_as_float(s[0]) + __uint_as_float(s[1]);
}
__device__ __forceinline__ float red16(float a, float b) {
  auto s = __builtin_amdgcn_permlane16_swap(__float_as_uint(a), __float_as_uint(b), false, false);
  return __uint_as_float(s[0]) + __uint_as_float(s[1]);
}
__device__ __forceinline__ float red8(float a, float b) { return dpp_upd<ROR8, 0xC>(a, b) + dpp_upd<ROR8, 0x3>(b, a); }
__device__ __forceinline__ float red4m(float a, float b) { return dpp_upd<HMIR, 0xA>(a, b) + dpp_upd<HMIR, 0x5>(b, a); }
__device__ __forceinline__ float red2(float a, float b, int lane) { const float t = a + dpp<XOR2>(a), u = b + dpp<XOR2>(b); return (lane & 2) ? u : t; }
__device__ __forceinline__ float red1(float a, float b, int lane) { const float t = a + dpp<XOR1>(a), u = b + dpp<XOR1>(b); return (lane & 1) ? u : t; }

__device__ __forceinline__ void tr_reduce24(float (&a)[24], float (&o)[3]) {
  float b[12], c[6];
#pragma unroll
  for (int v = 0; v < 12; ++v) b[v] = red32(a[v], a[v + 12]);
#pragma unroll
  for (int v = 0; v < 6; ++v) c[v] = red16(b[v], b[v + 6]);
#pragma unroll
  for (int v = 0; v < 3; ++v) o[v] = red8(c[v], c[v + 3]);
}

struct Streams { rsrc_t rw, rk, rv; int vo_w, vo_kv; };

template <int N> __device__ __forceinline__ u32x4 issue(const Streams& s) {
  if constexpr (N < NW) {
    constexpr int t = N / 3, j = N % 3;
    return __builtin_amdgcn_raw_buffer_load_b128(s.rw, s.vo_w, t * 8 * HD * 2 + j * 128, 0);
  } else {
    constexpr int n = N - NW, blk = n / 48, rem = n % 48, isv = rem / 24, q = rem % 24, i = q / 3, j = q % 3;
    constexpr int off = (blk * 64 + i * 8) * DH * 2 + j * 128;
    if constexpr (isv) return __builtin_amdgcn_raw_buffer_load_b128(s.rv, s.vo_kv, off, AUX_NT);
    else return __builtin_amdgcn_raw_buffer_load_b128(s.rk, s.vo_kv, off, AUX_NT);
  }
}
struct Tail {
  rsrc_t rwo, rw1, rw2; int vo_wo, vo_1, vo_2; bool role1;
  const u64* fp; u64 flag;
  u32x2 wo[12]; u32x4 w[3]; u32x2 w2[3];
};
__device__ __forceinline__ void issue_tail(Tail& t) {
#pragma unroll
  for (int i = 0; i < 12; ++i) t.wo[i] = __builtin_amdgcn_raw_buffer_load_b64(t.rwo, t.vo_wo, i * 8 * DM * 2, 0);
#pragma unroll
  for (int i = 0; i < 3; ++i) t.w[i] = __builtin_amdgcn_raw_buffer_load_b128(t.rw1, t.vo_1, i * 256 * FF * 2, 0);
#pragma unroll
  for (int i = 0; i < 3; ++i) t.w2[i] = __builtin_amdgcn_raw_buffer_load_b64(t.rw2, t.vo_2, i * 512 * DM * 2, 0);
}
template <int N> __device__ __forceinline__ void refill(u32x4 (&ring)[RING], const Streams& s, Tail& t) {
  if constexpr (N + RING < NLOADS) ring[N % RING] = issue<N + RING>(s);
  else if constexpr (N + RING == NLOADS) issue_tail(t);
  if constexpr (N == NW + 2 * 48) t.flag = __hip_atomic_load(t.fp, __ATOMIC_RELAXED, __HIP_MEMORY_SCOPE_AGENT);
  __builtin_amdgcn_sched_barrier(0);
}
template <int N, int E> __device__ __forceinline__ void prologue(u32x4 (&ring)[RING], const Streams& s) {
  if constexpr (N < E) { ring[N] = issue<N>(s); prologue<N + 1, E>(ring, s); }
}
__device__ __forceinline__ void fma8(float* acc, const u32x4 w, const float p) {
#pragma unroll
  for (int k = 0; k < 4; ++k) {
    acc[2 * k] = fmaf(p, bflo(w[k]), acc[2 * k]);
    acc[2 * k + 1] = fmaf(p, bfhi(w[k]), acc[2 * k + 1]);
  }
}
template <int T> __device__ __forceinline__ void qproj(u32x4 (&ring)[RING], const Streams& s, Tail& tl, float (&acc)[24], const float (&xn)[10]) {
  if constexpr (T < 10) {
    fma8(acc + 0, ring[(T * 3 + 0) % RING], xn[T]);  refill<T * 3 + 0>(ring, s, tl);
    fma8(acc + 8, ring[(T * 3 + 1) % RING], xn[T]);  refill<T * 3 + 1>(ring, s, tl);
    fma8(acc + 16, ring[(T * 3 + 2) % RING], xn[T]); refill<T * 3 + 2>(ring, s, tl);
    qproj<T + 1>(ring, s, tl, acc, xn);
  }
}
template <int BASE, int I> __device__ __forceinline__ void qk(u32x4 (&ring)[RING], const Streams& s, Tail& tl, float (&xs)[8], const u32x4 (&qf)[3]) {
  if constexpr (I < 8) {
    float d = 0.f;
#pragma unroll
    for (int k = 0; k < 4; ++k) d = dot2(ring[(BASE + I * 3 + 0) % RING][k], qf[0][k], d);
    refill<BASE + I * 3 + 0>(ring, s, tl);
#pragma unroll
    for (int k = 0; k < 4; ++k) d = dot2(ring[(BASE + I * 3 + 1) % RING][k], qf[1][k], d);
    refill<BASE + I * 3 + 1>(ring, s, tl);
#pragma unroll
    for (int k = 0; k < 4; ++k) d = dot2(ring[(BASE + I * 3 + 2) % RING][k], qf[2][k], d);
    refill<BASE + I * 3 + 2>(ring, s, tl);
    xs[I] = d;
    qk<BASE, I + 1>(ring, s, tl, xs, qf);
  }
}
template <int BASE, int I> __device__ __forceinline__ void pv(u32x4 (&ring)[RING], const Streams& s, Tail& tl, float (&acc)[24], const float (&pb)[8]) {
  if constexpr (I < 8) {
    fma8(acc + 0, ring[(BASE + I * 3 + 0) % RING], pb[I]);  refill<BASE + I * 3 + 0>(ring, s, tl);
    fma8(acc + 8, ring[(BASE + I * 3 + 1) % RING], pb[I]);  refill<BASE + I * 3 + 1>(ring, s, tl);
    fma8(acc + 16, ring[(BASE + I * 3 + 2) % RING], pb[I]); refill<BASE + I * 3 + 2>(ring, s, tl);
    pv<BASE, I + 1>(ring, s, tl, acc, pb);
  }
}
template <int BLK> __device__ __forceinline__ void blocks(u32x4 (&ring)[RING], const Streams& s, Tail& tl, float (&acc)[24], const u32x4 (&qf)[3],
                                                          float& m, float& l, const int c) {
  if constexpr (BLK < NBLK) {
    constexpr int BASE = NW + BLK * 48;
    float xsx[8];
    qk<BASE, 0>(ring, s, tl, xsx, qf);
    float y[4], z[2];
#pragma unroll
    for (int i = 0; i < 4; ++i) y[i] = red4m(xsx[i], xsx[i + 4]);
#pragma unroll
    for (int i = 0; i < 2; ++i) z[i] = red2(y[i], y[i + 2], c);
    float sc = red1(z[0], z[1], c);
    sc = rbf(rbf(sc) * SCALE);
    const float bm = wave_max(sc);
    const float mn = fmaxf(m, bm);
    const float alpha = __builtin_amdgcn_exp2f((m - mn) * LOG2E);
    const float p = __builtin_amdgcn_exp2f((sc - mn) * LOG2E);
    l = fmaf(l, alpha, p);
    m = mn;
    if constexpr (BLK > 0) {
#pragma unroll
      for (int n = 0; n < 24; ++n) acc[n] *= alpha;
    }
    float pb[8];
    {
      const float a0 = dpp<0x00>(p), a1 = dpp<0x55>(p), a2 = dpp<0xAA>(p), a3 = dpp<0xFF>(p);
      pb[0] = dpp_upd<HMIR, 0xA>(a0, a0); pb[4] = dpp_upd<HMIR, 0x5>(a0, a0);
      pb[1] = dpp_upd<HMIR, 0xA>(a1, a1); pb[5] = dpp_upd<HMIR, 0x5>(a1, a1);
      pb[2] = dpp_upd<HMIR, 0xA>(a2, a2); pb[6] = dpp_upd<HMIR, 0x5>(a2, a2);
      pb[3] = dpp_upd<HMIR, 0xA>(a3, a3); pb[7] = dpp_upd<HMIR, 0x5>(a3, a3);
    }
    pv<BASE + 24, 0>(ring, s, tl, acc, pb);
    blocks<BLK + 1>(ring, s, tl, acc, qf, m, l, c);
  }
}

__device__ __forceinline__ u32x4 ld_g2(rsrc_t r, u32 off) { return __builtin_amdgcn_raw_buffer_load_b128(r, off, 0, AUX_SC1); }
__device__ __forceinline__ void st_g2(rsrc_t r, u32 off, u32x4 v) { __builtin_amdgcn_raw_buffer_store_b128(v, r, off, 0, AUX_SC1); }
__device__ __forceinline__ bool tags_ok(const u32x4 v) { return v[1] == TAG && v[3] == TAG; }
#define POLL_FENCE() asm volatile("" ::: "memory")
#define STAMP(i) do { } while (0)

__global__ void __launch_bounds__(512, 2)
fused_kernel(const u16* __restrict__ x, const u16* __restrict__ kc, const u16* __restrict__ vc,
             const u16* __restrict__ wq, const u16* __restrict__ wo, const u16* __restrict__ w1, const u16* __restrict__ b1,
             const u16* __restrict__ w2, const u16* __restrict__ b2, unsigned char* __restrict__ ws, u16* __restrict__ out) {
  __shared__ __attribute__((aligned(16))) float s_red[WAVES][DH];
  __shared__ __attribute__((aligned(16))) float s_h[2][DM];
  __shared__ __attribute__((aligned(16))) float s_y[WAVES][40];
  __shared__ __attribute__((aligned(16))) u16 s_q[DH];
  __shared__ float s_m[WAVES], s_l[WAVES];

  const int bid = blockIdx.x;
  const int g = bid & 7, jj = bid >> 3;
  const int h = g * 4 + (jj >> 3);
  const int b = (jj >> 2) & 1, sp = jj & 3;
  const int tid = threadIdx.x, lane = tid & 63;
  const int wave = __builtin_amdgcn_readfirstlane(tid >> 6);
  const int r = lane >> 3, c = lane & 7;
  const bool role1 = jj < 12;

  STAMP(0);
  const rsrc_t rws = __builtin_amdgcn_make_buffer_rsrc((void*)ws, 0, WS_BYTES, 0x00020000);
  const u32 my_h1 = OFF_H1 + (u32)(g * 32 + jj) * H1_STRIDE;
  if (wave == 7) {
    const u32x4 z4 = {0u, 0u, 0u, 0u};
    st_g2(rws, my_h1 + lane * 16, z4);
    if (lane < 40) st_g2(rws, my_h1 + 1024 + lane * 16, z4);
    if (lane < 20) st_g2(rws, OFF_H2 + (u32)((g * 2 + lane / 10) * DM + 20 * jj + 2 * (lane % 10)) * 8, z4);
    if (role1 && lane < 8) st_g2(rws, OFF_H3 + (u32)(192 * g + 16 * jj + 2 * lane) * 8, z4);
  }
  const u16* xb = x + b * DM;
  u32 xs[5];
#pragma unroll
  for (int i = 0; i < 5; ++i) xs[i] = reinterpret_cast<const u32*>(xb)[lane + 64 * i];
  u32 xr[10];
#pragma unroll
  for (int t = 0; t < 10; ++t) xr[t] = xb[80 * wave + 8 * t + r];

  Streams s;
  Tail tl;
  {
    const u16* wqp = wq + (size_t)(80 * wave) * HD + h * DH;
    const size_t kvoff = ((size_t)(b * NH + h) * SK + sp * KEYS_WG + wave * KEYS_WAVE) * DH;
    s.rw = __builtin_amdgcn_make_buffer_rsrc((void*)wqp, 0, (79 * HD + DH) * 2, 0x00020000);
    s.rk = __builtin_amdgcn_make_buffer_rsrc((void*)(kc + kvoff), 0, KEYS_WAVE * DH * 2, 0x00020000);
    s.rv = __builtin_amdgcn_make_buffer_rsrc((void*)(vc + kvoff), 0, KEYS_WAVE * DH * 2, 0x00020000);
    s.vo_w = r * HD * 2 + c * 16;
    s.vo_kv = r * DH * 2 + c * 16;
    tl.rwo = __builtin_amdgcn_make_buffer_rsrc((void*)(wo + (size_t)(768 * g + 96 * wave) * DM), 0, 96 * DM * 2, 0x00020000);
    tl.vo_wo = (c < 5) ? (r * DM + 20 * jj + 4 * c) * 2 : 0x40000000;
    tl.role1 = role1;
    tl.fp = reinterpret_cast<const u64*>(ws + OFF_FLAGS) + (tid & 255);
    tl.rw1 = __builtin_amdgcn_make_buffer_rsrc((void*)w1, 0, DM * FF * 2, 0x00020000);
    tl.vo_1 = role1 ? ((32 * wave + (lane & 31)) * FF + 192 * g + 16 * jj + 8 * (lane >> 5)) * 2 : 0x40000000;
    tl.rw2 = __builtin_amdgcn_make_buffer_rsrc((void*)w2, 0, FF * DM * 2, 0x00020000);
    tl.vo_2 = role1 ? 0x40000000 : (tid * DM + 80 * g + 4 * (jj - 12)) * 2;
  }
  u32x4 ring[RING];
  __builtin_amdgcn_sched_barrier(0);
  prologue<0, RING>(ring, s);
  const u64 epoch = __hip_atomic_load(reinterpret_cast<u64*>(ws + OFF_EPOCH), __ATOMIC_RELAXED, __HIP_MEMORY_SCOPE_AGENT);
  __builtin_amdgcn_sched_barrier(0);

  float xn[10];
  {
    float sum = 0.f;
#pragma unroll
    for (int i = 0; i < 5; ++i) sum += bflo(xs[i]) + bfhi(xs[i]);
    sum = wave_sum(sum);
    const float mean = sum * (1.0f / DM);
    float sq = 0.f;
#pragma unroll
    for (int i = 0; i < 5; ++i) { float a = bflo(xs[i]) - mean, bb = bfhi(xs[i]) - mean; sq += a * a + bb * bb; }
    sq = wave_sum(sq);
    const float mb = rbf(mean), vb = rbf(sq * (1.0f / DM));
    const float den = rbf(sqrtf(rbf(vb + 1e-5f)));
#pragma unroll
    for (int t = 0; t < 10; ++t) xn[t] = rbf(rbf(bf2f(xr[t]) - mb) / den);
  }
  float acc[24];
#pragma unroll
  for (int n = 0; n < 24; ++n) acc[n] = 0.f;
  qproj<0>(ring, s, tl, acc, xn);

  const u64 tag64 = epoch + 1ull;
  asm volatile("s_waitcnt vmcnt(%0)" ::"n"(RING) : "memory");
  __builtin_amdgcn_raw_buffer_store_b64(u32x2{(u32)tag64, (u32)(tag64 >> 32)}, rws, (tid == 448) ? OFF_FLAGS + bid * 8 : 0x40000000u, 0, AUX_SC1);
  __builtin_amdgcn_sched_barrier(0);

  const int n0 = 12 * (lane >> 5) + 6 * ((lane >> 4) & 1) + 3 * ((lane >> 3) & 1);
  {
    float o3[3];
    tr_reduce24(acc, o3);
#pragma unroll
    for (int v = 0; v < 3; ++v) { const int n = n0 + v; s_red[wave][(n >> 3) * 64 + c * 8 + (n & 7)] = o3[v]; }
  }
  __syncthreads();
  if (tid < DH) {
    float q = 0.f;
#pragma unroll
    for (int w = 0; w < WAVES; ++w) q += s_red[w][tid];
    s_q[tid] = (u16)f2bf(q);
  }
  __syncthreads();
  u32x4 qf[3];
#pragma unroll
  for (int j = 0; j < 3; ++j) qf[j] = *reinterpret_cast<const u32x4*>(&s_q[j * 64 + c * 8]);

  STAMP(1);
  float m = -INFINITY, l = 0.f;
#pragma unroll
  for (int n = 0; n < 24; ++n) acc[n] = 0.f;
  blocks<0>(ring, s, tl, acc, qf, m, l, c);

  STAMP(2);
  l = wave_sum(l);
  {
    float o3[3];
    tr_reduce24(acc, o3);
#pragma unroll
    for (int v = 0; v < 3; ++v) { const int n = n0 + v; s_red[wave][(n >> 3) * 64 + c * 8 + (n & 7)] = o3[v]; }
    if (lane == 0) { s_m[wave] = m; s_l[wave] = l; }
  }
  if (!__all(tl.flag == tag64)) {
    for (int spin = 0; spin < SPIN_MAX; ++spin) {
      const u64 f = __hip_atomic_load(tl.fp, __ATOMIC_RELAXED, __HIP_MEMORY_SCOPE_AGENT);
      if (__all(f == tag64)) break;
      __builtin_amdgcn_s_sleep(4);
    }
  }
  __syncthreads();
  if (tid <= 96) {
    float M = s_m[0];
#pragma unroll
    for (int w = 1; w < WAVES; ++w) M = fmaxf(M, s_m[w]);
    float L = 0.f, o0 = 0.f, o1 = 0.f;
    const int d = (tid < 96) ? 2 * tid : 0;
#pragma unroll
    for (int w = 0; w < WAVES; ++w) {
      const float e = __builtin_amdgcn_exp2f((s_m[w] - M) * LOG2E);
      const f32x2 a = *reinterpret_cast<const f32x2*>(&s_red[w][d]);
      L = fmaf(e, s_l[w], L);
      o0 = fmaf(e, a[0], o0);
      o1 = fmaf(e, a[1], o1);
    }
    if (tid == 96) { o0 = M; o1 = L; }
    st_g2(rws, my_h1 + tid * 16, u32x4{fbits(o0), TAG, fbits(o1), TAG});
  }
  __syncthreads();
  STAMP(3);

  float* s_o = &s_red[0][0];
  {
    u32x4 pa[4], pb2[4];
    const u32 base = OFF_H1 + (u32)(g * 32 + wave * 4) * H1_STRIDE;
    const u32 oa = lane * 16, ob = (lane < 33) ? 1024 + lane * 16 : 0x40000000u;
    unsigned done = 0;
    for (int spin = 0; spin < SPIN_MAX; ++spin) {
      POLL_FENCE();
#pragma unroll
      for (int q = 0; q < 4; ++q) {
        if (!(done & (1u << q))) {
          pa[q] = ld_g2(rws, base + q * H1_STRIDE + oa);
          pb2[q] = ld_g2(rws, (lane < 33) ? base + q * H1_STRIDE + ob : ob);
        }
      }
#pragma unroll
      for (int q = 0; q < 4; ++q) {
        const bool ok = tags_ok(pa[q]) && (lane >= 33 || tags_ok(pb2[q]));
        if (__all(ok)) done |= 1u << q;
      }
      if (done == 15u) break;
      __builtin_amdgcn_s_sleep(2);
    }
    float ms[4], ls[4];
#pragma unroll
    for (int q = 0; q < 4; ++q) {
      ms[q] = bitsf(__builtin_amdgcn_readlane(pb2[q][0], 32));
      ls[q] = bitsf(__builtin_amdgcn_readlane(pb2[q][2], 32));
    }
    const float M = fmaxf(fmaxf(ms[0], ms[1]), fmaxf(ms[2], ms[3]));
    float L = 0.f, a0 = 0.f, a1 = 0.f, b0 = 0.f, b1v = 0.f;
#pragma unroll
    for (int q = 0; q < 4; ++q) {
      const float e = __builtin_amdgcn_exp2f((ms[q] - M) * LOG2E);
      L = fmaf(e, ls[q], L);
      a0 = fmaf(e, bitsf(pa[q][0]), a0); a1 = fmaf(e, bitsf(pa[q][2]), a1);
      b0 = fmaf(e, bitsf(pb2[q][0]), b0); b1v = fmaf(e, bitsf(pb2[q][2]), b1v);
    }
    const int rowb = (wave >> 1) * DH, bb = wave & 1;
    s_o[(rowb + 2 * lane) * 2 + bb] = rbf(a0 / L);
    s_o[(rowb + 2 * lane + 1) * 2 + bb] = rbf(a1 / L);
    if (lane < 32) {
      s_o[(rowb + 128 + 2 * lane) * 2 + bb] = rbf(b0 / L);
      s_o[(rowb + 128 + 2 * lane + 1) * 2 + bb] = rbf(b1v / L);
    }
  }
  __syncthreads();
  STAMP(4);
  {
    float y[8];
#pragma unroll
    for (int e = 0; e < 8; ++e) y[e] = 0.f;
#pragma unroll
    for (int i = 0; i < 12; ++i) {
      const f32x2 o = *reinterpret_cast<const f32x2*>(&s_o[(96 * wave + 8 * i + r) * 2]);
      const float w0 = bflo(tl.wo[i][0]), w1v = bfhi(tl.wo[i][0]), w2v = bflo(tl.wo[i][1]), w3 = bfhi(tl.wo[i][1]);
      y[0] = fmaf(o[0], w0, y[0]); y[1] = fmaf(o[0], w1v, y[1]); y[2] = fmaf(o[0], w2v, y[2]); y[3] = fmaf(o[0], w3, y[3]);
      y[4] = fmaf(o[1], w0, y[4]); y[5] = fmaf(o[1], w1v, y[5]); y[6] = fmaf(o[1], w2v, y[6]); y[7] = fmaf(o[1], w3, y[7]);
    }
    float u[4], v[2];
#pragma unroll
    for (int e = 0; e < 4; ++e) u[e] = red32(y[e], y[e + 4]);
#pragma unroll
    for (int e = 0; e < 2; ++e) v[e] = red16(u[e], u[e + 2]);
    const float t = red8(v[0], v[1]);
    if (c < 5) s_y[wave][(lane >> 5) * 20 + 4 * c + 2 * ((lane >> 4) & 1) + ((lane >> 3) & 1)] = t;
  }
  __syncthreads();
  if (tid < 20) {
    float y0 = 0.f, y1 = 0.f;
#pragma unroll
    for (int w = 0; w < WAVES; ++w) { const f32x2 a = *reinterpret_cast<const f32x2*>(&s_y[w][2 * tid]); y0 += a[0]; y1 += a[1]; }
    st_g2(rws, OFF_H2 + (u32)((g * 2 + tid / 10) * DM + 20 * jj + 2 * (tid % 10)) * 8, u32x4{fbits(y0), TAG, fbits(y1), TAG});
  }

  STAMP(5);
  if (role1) {
    const u32 xa = reinterpret_cast<const u32*>(x)[tid];
    const u32 xbv = (tid < 128) ? reinterpret_cast<const u32*>(x)[512 + tid] : 0u;
    const float bias0 = bf2f(b1[192 * g + 16 * jj + (tid & 15)]);
    {
      u32x4 pa[8], pb2[8];
      const u32 oa = OFF_H2 + tid * 16, ob = (tid < 128) ? OFF_H2 + (512 + tid) * 16 : 0x40000000u;
      for (int spin = 0; spin < SPIN_MAX; ++spin) {
        POLL_FENCE();
#pragma unroll
        for (int q = 0; q < 8; ++q) pa[q] = ld_g2(rws, oa + q * (2 * DM * 8));
        if (wave < 2) {
#pragma unroll
          for (int q = 0; q < 8; ++q) pb2[q] = ld_g2(rws, ob + q * (2 * DM * 8));
        }
        bool ok = true;
#pragma unroll
        for (int q = 0; q < 8; ++q) ok = ok && tags_ok(pa[q]);
        if (wave < 2) {
#pragma unroll
          for (int q = 0; q < 8; ++q) ok = ok && tags_ok(pb2[q]);
        }
        if (__all(ok)) break;
        __builtin_amdgcn_s_sleep(2);
      }
      float y0 = 0.f, y1 = 0.f;
#pragma unroll
      for (int q = 0; q < 8; ++q) { y0 += bitsf(pa[q][0]); y1 += bitsf(pa[q][2]); }
      float* sh = &s_h[0][0];
      *reinterpret_cast<f32x2*>(&sh[2 * tid]) = f32x2{rbf(bflo(xa) + rbf(y0)), rbf(bfhi(xa) + rbf(y1))};
      if (wave < 2) {
        y0 = 0.f; y1 = 0.f;
#pragma unroll
        for (int q = 0; q < 8; ++q) { y0 += bitsf(pb2[q][0]); y1 += bitsf(pb2[q][2]); }
        *reinterpret_cast<f32x2*>(&sh[1024 + 2 * tid]) = f32x2{rbf(bflo(xbv) + rbf(y0)), rbf(bfhi(xbv) + rbf(y1))};
      }
    }
    __syncthreads();
    STAMP(6);
    float mb[2], den[2];
#pragma unroll
    for (int bb = 0; bb < 2; ++bb) {
      f32x2 hv[5];
#pragma unroll
      for (int i = 0; i < 5; ++i) hv[i] = *reinterpret_cast<const f32x2*>(&s_h[bb][2 * (lane + 64 * i)]);
      float sum = 0.f;
#pragma unroll
      for (int i = 0; i < 5; ++i) sum += hv[i][0] + hv[i][1];
      const float mean = wave_sum(sum) * (1.0f / DM);
      float sq = 0.f;
#pragma unroll
      for (int i = 0; i < 5; ++i) { const float a = hv[i][0] - mean, e = hv[i][1] - mean; sq += a * a + e * e; }
      sq = wave_sum(sq);
      mb[bb] = rbf(mean);
      den[bb] = rbf(sqrtf(rbf(rbf(sq * (1.0f / DM)) + 1e-5f)));
    }
    float a[16];
#pragma unroll
    for (int e = 0; e < 16; ++e) a[e] = 0.f;
#pragma unroll
    for (int i = 0; i < 3; ++i) {
      const int row = 32 * wave + (lane & 31) + 256 * i;
      const int rc = row < DM ? row : 0;
      const float n0v = rbf(rbf(s_h[0][rc] - mb[0]) / den[0]);
      const float n1v = rbf(rbf(s_h[1][rc] - mb[1]) / den[1]);
      fma8(a, tl.w[i], n0v);
      fma8(a + 8, tl.w[i], n1v);
    }
    float u[8], v[4], w2r[2];
#pragma unroll
    for (int e = 0; e < 8; ++e) u[e] = red16(a[e], a[e + 8]);
#pragma unroll
    for (int e = 0; e < 4; ++e) v[e] = red8(u[e], u[e + 4]);
#pragma unroll
    for (int e = 0; e < 2; ++e) w2r[e] = red4m(v[e], v[e + 2]);
    float t = red2(w2r[0], w2r[1], lane);
    t += dpp<XOR1>(t);
    float* s_p = &s_y[0][0];
    if ((lane & 1) == 0) s_p[wave * 32 + ((lane >> 4) & 1) * 16 + (lane >> 5) * 8 + ((lane >> 3) & 1) * 4 + ((lane >> 2) & 1) * 2 + ((lane >> 1) & 1)] = t;
    __syncthreads();
    if (tid < 32) {
      float sacc = 0.f;
#pragma unroll
      for (int w = 0; w < WAVES; ++w) sacc += s_p[w * 32 + tid];
      const float hh = rbf(rbf(sacc) + bias0);
      const float rr = hh > 0.f ? hh : 0.f;
      const u32 hb = f2bf(rr * rr);
      const u32 other = __shfl_xor(hb, 16);
      const u32 pk = hb | (other << 16);
      const u32 pk1 = __shfl_xor(pk, 1);
      if (tid < 16 && (tid & 1) == 0) st_g2(rws, OFF_H3 + (u32)(192 * g + 16 * jj + tid) * 8, u32x4{pk, TAG, pk1, TAG});
    }
    STAMP(7);
  } else {
    const int col0 = 80 * g + 4 * (jj - 12);
    float hres = 0.f, bias = 0.f;
    if (wave == 0) {
      const int q = lane >> 2, bb = (lane >> 1) & 1, pr = lane & 1;
      const u32 off = (lane < 32) ? OFF_H2 + (u32)((q * 2 + bb) * DM + col0 + 2 * pr) * 8 : 0x40000000u;
      const u32 xw = reinterpret_cast<const u32*>(x)[(bb * DM + col0 + 2 * pr) >> 1];
      u32x4 pv4;
      for (int spin = 0; spin < SPIN_MAX; ++spin) {
        POLL_FENCE();
        pv4 = ld_g2(rws, off);
        if (__all(lane >= 32 || tags_ok(pv4))) break;
        __builtin_amdgcn_s_sleep(2);
      }
      float y0 = 0.f, y1 = 0.f;
#pragma unroll
      for (int qq = 0; qq < 8; ++qq) {
        y0 += bitsf(__shfl(pv4[0], qq * 4 + (lane & 3)));
        y1 += bitsf(__shfl(pv4[2], qq * 4 + (lane & 3)));
      }
      const float h0 = rbf(bflo(xw) + rbf(y0)), h1 = rbf(bfhi(xw) + rbf(y1));
      const int src = ((lane >> 2) & 1) * 2 + ((lane >> 1) & 1);
      const float sh0 = __shfl(h0, src), sh1 = __shfl(h1, src);
      hres = (lane & 1) ? sh1 : sh0;
      bias = bf2f(b2[col0 + (lane & 3)]);
    }
    STAMP(6);
    u64 hv[3];
    const u64* hp = reinterpret_cast<const u64*>(ws + OFF_H3) + tid;
    for (int spin = 0; spin < SPIN_MAX; ++spin) {
      bool ok = true;
#pragma unroll
      for (int i = 0; i < 3; ++i) { hv[i] = __hip_atomic_load(hp + 512 * i, __ATOMIC_RELAXED, __HIP_MEMORY_SCOPE_AGENT); ok = ok && ((u32)(hv[i] >> 32) == TAG); }
      if (__all(ok)) break;
      __builtin_amdgcn_s_sleep(2);
    }
    STAMP(7);
    float y[8];
#pragma unroll
    for (int e = 0; e < 8; ++e) y[e] = 0.f;
#pragma unroll
    for (int i = 0; i < 3; ++i) {
      const float h0 = bflo((u32)hv[i]), h1 = bfhi((u32)hv[i]);
      const float w0 = bflo(tl.w2[i][0]), w1v = bfhi(tl.w2[i][0]), w2v = bflo(tl.w2[i][1]), w3 = bfhi(tl.w2[i][1]);
      y[0] = fmaf(h0, w0, y[0]); y[1] = fmaf(h0, w1v, y[1]); y[2] = fmaf(h0, w2v, y[2]); y[3] = fmaf(h0, w3, y[3]);
      y[4] = fmaf(h1, w0, y[4]); y[5] = fmaf(h1, w1v, y[5]); y[6] = fmaf(h1, w2v, y[6]); y[7] = fmaf(h1, w3, y[7]);
    }
    float u[4], v[2];
#pragma unroll
    for (int e = 0; e < 4; ++e) u[e] = red32(y[e], y[e + 4]);
#pragma unroll
    for (int e = 0; e < 2; ++e) v[e] = red16(u[e], u[e + 2]);
    float t = red8(v[0], v[1]);
    t += dpp<HMIR>(t);
    t += dpp<XOR2>(t);
    t += dpp<XOR1>(t);
    __syncthreads();
    float* s_p = &s_y[0][0];
    if (c == 0) s_p[wave * 8 + r] = t;
    __syncthreads();
    if (tid < 8) {
      float sacc = 0.f;
#pragma unroll
      for (int w = 0; w < WAVES; ++w) sacc += s_p[w * 8 + tid];
      const float o = rbf(rbf(sacc) + bias);
      out[(tid >> 2) * DM + col0 + (tid & 3)] = (u16)f2bf(hres + o);
    }
    STAMP(8);
    if (bid == 255 && tid == 0) __hip_atomic_store(reinterpret_cast<u64*>(ws + OFF_EPOCH), tag64, __ATOMIC_RELAXED, __HIP_MEMORY_SCOPE_AGENT);
  }
}

extern "C" void kernel_launch(void* const* d_in, const int* in_sizes, int n_in,
                              void* d_out, int out_size, void* d_ws, size_t ws_size,
                              hipStream_t stream) {
  fused_kernel<<<256, 512, 0, stream>>>((const u16*)d_in[0], (const u16*)d_in[1], (const u16*)d_in[2], (const u16*)d_in[3],
                                        (const u16*)d_in[4], (const u16*)d_in[6], (const u16*)d_in[7], (const u16*)d_in[8],
                                        (const u16*)d_in[9], (unsigned char*)d_ws, (u16*)d_out);
}
```

```cpp
#include <hip/hip_runtime.h>
#include <hip/hip_bf16.h>
#include <stdint.h>

typedef unsigned int u32;
typedef unsigned short u16;
typedef unsigned long long u64;
using u32x4 = __attribute__((ext_vector_type(4))) unsigned;
using u32x2 = __attribute__((ext_vector_type(2))) unsigned;
using f32x2 = __attribute__((ext_vector_type(2))) float;
using bf16x2v = __attribute__((ext_vector_type(2))) __bf16;
typedef __amdgpu_buffer_rsrc_t rsrc_t;

constexpr int DM = 640, NH = 32, SK = 8192, DH = 192, FF = 1536;
constexpr int HD = NH * DH;
constexpr int NSPLIT = 4;
constexpr int KEYS_WG = SK / NSPLIT;
constexpr int WAVES = 8;
constexpr int KEYS_WAVE = KEYS_WG / WAVES;
constexpr int NBLK = KEYS_WAVE / 64;
constexpr float SCALE = 0.07216878364870322f;
constexpr float LOG2E = 1.4426950408889634f;

#define RING 16
constexpr int NW = 30;
constexpr int NLOADS = NW + NBLK * 48;

constexpr u32 OFF_EPOCH = 0;
constexpr u32 OFF_FLAGS = 1024;
constexpr u32 OFF_H1 = 4096;
constexpr u32 H1_STRIDE = 1664;
constexpr u32 OFF_H2 = OFF_H1 + 256 * H1_STRIDE;
constexpr u32 OFF_H3 = OFF_H2 + 8 * 2 * DM * 8;
constexpr u32 WS_BYTES = OFF_H3 + FF * 8;
constexpr u32 TAG = 0x5bd1e995u;
constexpr int SPIN_MAX = 1 << 18;
constexpr int AUX_NT = 2, AUX_SC1 = 16;

__device__ __forceinline__ float bf2f(u32 h) { return __builtin_bit_cast(float, h << 16); }
__device__ __forceinline__ float bflo(u32 w) { return __builtin_bit_cast(float, w << 16); }
__device__ __forceinline__ float bfhi(u32 w) { return __builtin_bit_cast(float, w & 0xffff0000u); }
__device__ __forceinline__ float rbf(float x) { return __bfloat162float(__float2bfloat16(x)); }
__device__ __forceinline__ u32 f2bf(float x) { return (u32)__builtin_bit_cast(u16, __float2bfloat16(x)); }
__device__ __forceinline__ u32 fbits(float x) { return __builtin_bit_cast(u32, x); }
__device__ __forceinline__ float bitsf(u32 x) { return __builtin_bit_cast(float, x); }

__device__ __forceinline__ float dot2(u32 k, u32 q, float acc) {
  return __builtin_amdgcn_fdot2_f32_bf16(__builtin_bit_cast(bf16x2v, k), __builtin_bit_cast(bf16x2v, q), acc, false);
}
template <int CTRL> __device__ __forceinline__ float dpp(float x) {
  return __builtin_bit_cast(float, __builtin_amdgcn_mov_dpp(__builtin_bit_cast(int, x), CTRL, 0xf, 0xf, true));
}
template <int CTRL, int BANK> __device__ __forceinline__ float dpp_upd(float old, float x) {
  return __builtin_bit_cast(float, __builtin_amdgcn_update_dpp(__builtin_bit_cast(int, old), __builtin_bit_cast(int, x), CTRL, 0xf, BANK, false));
}
constexpr int XOR1 = 0xB1, XOR2 = 0x4E, HMIR = 0x141, ROR8 = 0x128;

__device__ __forceinline__ float wave_max(float x) {
  x = fmaxf(x, dpp<XOR1>(x));
  x = fmaxf(x, dpp<XOR2>(x));
  x = fmaxf(x, dpp<HMIR>(x));
  x = fmaxf(x, dpp<ROR8>(x));
  auto s = __builtin_amdgcn_permlane16_swap(__float_as_uint(x), __float_as_uint(x), false, false);
  x = fmaxf(__uint_as_float(s[0]), __uint_as_float(s[1]));
  auto t = __builtin_amdgcn_permlane32_swap(__float_as_uint(x), __float_as_uint(x), false, false);
  return fmaxf(__uint_as_float(t[0]), __uint_as_float(t[1]));
}
__device__ __forceinline__ float wave_sum(float x) {
  x += dpp<XOR1>(x);
  x += dpp<XOR2>(x);
  x += dpp<HMIR>(x);
  x += dpp<ROR8>(x);
  auto s = __builtin_amdgcn_permlane16_swap(__float_as_uint(x), __float_as_uint(x), false, false);
  x = __uint_as_float(s[0]) + __uint_as_float(s[1]);
  auto t = __builtin_amdgcn_permlane32_swap(__float_as_uint(x), __float_as_uint(x), false, false);
  return __uint_as_float(t[0]) + __uint_as_float(t[1]);
}
__device__ __forceinline__ float red32(float a, float b) {
  auto s = __builtin_amdgcn_permlane32_swap(__float_as_uint(a), __float_as_uint(b), false, false);
  return __uint_as_float(s[0]) + __uint_as_float(s[1]);
}
__device__ __forceinline__ float red16(float a, float b) {
  auto s = __builtin_amdgcn_permlane16_swap(__float_as_uint(a), __float_as_uint(b), false, false);
  return __uint_as_float(s[0]) + __uint_as_float(s[1]);
}
__device__ __forceinline__ float red8(float a, float b) { return dpp_upd<ROR8, 0xC>(a, b) + dpp_upd<ROR8, 0x3>(b, a); }
__device__ __forceinline__ float red4m(float a, float b) { return dpp_upd<HMIR, 0xA>(a, b) + dpp_upd<HMIR, 0x5>(b, a); }
__device__ __forceinline__ float red2(float a, float b, int lane) { const float t = a + dpp<XOR2>(a), u = b + dpp<XOR2>(b); return (lane & 2) ? u : t; }
__device__ __forceinline__ float red1(float a, float b, int lane) { const float t = a + dpp<XOR1>(a), u = b + dpp<XOR1>(b); return (lane & 1) ? u : t; }

__device__ __forceinline__ void tr_reduce24(float (&a)[24], float (&o)[3]) {
  float b[12], c[6];
#pragma unroll
  for (int v = 0; v < 12; ++v) b[v] = red32(a[v], a[v + 12]);
#pragma unroll
  for (int v = 0; v < 6; ++v) c[v] = red16(b[v], b[v + 6]);
#pragma unroll
  for (int v = 0; v < 3; ++v) o[v] = red8(c[v], c[v + 3]);
}

struct Streams { rsrc_t rw, rk, rv; int vo_w, vo_kv; };

template <int N> __device__ __forceinline__ u32x4 issue(const Streams& s) {
  if constexpr (N < NW) {
    constexpr int t = N / 3, j = N % 3;
    return __builtin_amdgcn_raw_buffer_load_b128(s.rw, s.vo_w, t * 8 * HD * 2 + j * 128, AUX_NT);
  } else {
    constexpr int n = N - NW, blk = n / 48, rem = n % 48, isv = rem / 24, q = rem % 24, i = q / 3, j = q % 3;
    constexpr int off = (blk * 64 + i * 8) * DH * 2 + j * 128;
    if constexpr (isv) return __builtin_amdgcn_raw_buffer_load_b128(s.rv, s.vo_kv, off, AUX_NT);
    else return __builtin_amdgcn_raw_buffer_load_b128(s.rk, s.vo_kv, off, AUX_NT);
  }
}
struct Tail {
  rsrc_t rwo, rw1, rw2; int vo_wo, vo_1, vo_2; bool role1;
  const u64* fp; u64 flag;
  u32x2 wo[12]; u32x4 w[3]; u32x2 w2[3];
};
__device__ __forceinline__ void issue_tail(Tail& t) {
#pragma unroll
  for (int i = 0; i < 12; ++i) t.wo[i] = __builtin_amdgcn_raw_buffer_load_b64(t.rwo, t.vo_wo, i * 8 * DM * 2, AUX_NT);
#pragma unroll
  for (int i = 0; i < 3; ++i) t.w[i] = __builtin_amdgcn_raw_buffer_load_b128(t.rw1, t.vo_1, i * 256 * FF * 2, AUX_NT);
#pragma unroll
  for (int i = 0; i < 3; ++i) t.w2[i] = __builtin_amdgcn_raw_buffer_load_b64(t.rw2, t.vo_2, i * 512 * DM * 2, AUX_NT);
}
template <int N> __device__ __forceinline__ void refill(u32x4 (&ring)[RING], const Streams& s, Tail& t) {
  if constexpr (N + RING < NLOADS) ring[N % RING] = issue<N + RING>(s);
  else if constexpr (N + RING == NLOADS) issue_tail(t);
  if constexpr (N == NW + 2 * 48) t.flag = __hip_atomic_load(t.fp, __ATOMIC_RELAXED, __HIP_MEMORY_SCOPE_AGENT);
  __builtin_amdgcn_sched_barrier(0);
}
template <int N, int E> __device__ __forceinline__ void prologue(u32x4 (&ring)[RING], const Streams& s) {
  if constexpr (N < E) { ring[N] = issue<N>(s); prologue<N + 1, E>(ring, s); }
}
__device__ __forceinline__ void fma8(float* acc, const u32x4 w, const float p) {
#pragma unroll
  for (int k = 0; k < 4; ++k) {
    acc[2 * k] = fmaf(p, bflo(w[k]), acc[2 * k]);
    acc[2 * k + 1] = fmaf(p, bfhi(w[k]), acc[2 * k + 1]);
  }
}
template <int T> __device__ __forceinline__ void qproj(u32x4 (&ring)[RING], const Streams& s, Tail& tl, float (&acc)[24], const float (&xn)[10]) {
  if constexpr (T < 10) {
    fma8(acc + 0, ring[(T * 3 + 0) % RING], xn[T]);  refill<T * 3 + 0>(ring, s, tl);
    fma8(acc + 8, ring[(T * 3 + 1) % RING], xn[T]);  refill<T * 3 + 1>(ring, s, tl);
    fma8(acc + 16, ring[(T * 3 + 2) % RING], xn[T]); refill<T * 3 + 2>(ring, s, tl);
    qproj<T + 1>(ring, s, tl, acc, xn);
  }
}
template <int BASE, int I> __device__ __forceinline__ void qk(u32x4 (&ring)[RING], const Streams& s, Tail& tl, float (&xs)[8], const u32x4 (&qf)[3]) {
  if constexpr (I < 8) {
    float d = 0.f;
#pragma unroll
    for (int k = 0; k < 4; ++k) d = dot2(ring[(BASE + I * 3 + 0) % RING][k], qf[0][k], d);
    refill<BASE + I * 3 + 0>(ring, s, tl);
#pragma unroll
    for (int k = 0; k < 4; ++k) d = dot2(ring[(BASE + I * 3 + 1) % RING][k], qf[1][k], d);
    refill<BASE + I * 3 + 1>(ring, s, tl);
#pragma unroll
    for (int k = 0; k < 4; ++k) d = dot2(ring[(BASE + I * 3 + 2) % RING][k], qf[2][k], d);
    refill<BASE + I * 3 + 2>(ring, s, tl);
    xs[I] = d;
    qk<BASE, I + 1>(ring, s, tl, xs, qf);
  }
}
template <int BASE, int I> __device__ __forceinline__ void pv(u32x4 (&ring)[RING], const Streams& s, Tail& tl, float (&acc)[24], const float (&pb)[8]) {
  if constexpr (I < 8) {
    fma8(acc + 0, ring[(BASE + I * 3 + 0) % RING], pb[I]);  refill<BASE + I * 3 + 0>(ring, s, tl);
    fma8(acc + 8, ring[(BASE + I * 3 + 1) % RING], pb[I]);  refill<BASE + I * 3 + 1>(ring, s, tl);
    fma8(acc + 16, ring[(BASE + I * 3 + 2) % RING], pb[I]); refill<BASE + I * 3 + 2>(ring, s, tl);
    pv<BASE, I + 1>(ring, s, tl, acc, pb);
  }
}
template <int BLK> __device__ __forceinline__ void blocks(u32x4 (&ring)[RING], const Streams& s, Tail& tl, float (&acc)[24], const u32x4 (&qf)[3],
                                                          float& m, float& l, const int c) {
  if constexpr (BLK < NBLK) {
    constexpr int BASE = NW + BLK * 48;
    float xsx[8];
    qk<BASE, 0>(ring, s, tl, xsx, qf);
    float y[4], z[2];
#pragma unroll
    for (int i = 0; i < 4; ++i) y[i] = red4m(xsx[i], xsx[i + 4]);
#pragma unroll
    for (int i = 0; i < 2; ++i) z[i] = red2(y[i], y[i + 2], c);
    float sc = red1(z[0], z[1], c);
    sc = rbf(rbf(sc) * SCALE);
    const float bm = wave_max(sc);
    const float mn = fmaxf(m, bm);
    const float alpha = __builtin_amdgcn_exp2f((m - mn) * LOG2E);
    const float p = __builtin_amdgcn_exp2f((sc - mn) * LOG2E);
    l = fmaf(l, alpha, p);
    m = mn;
    if constexpr (BLK > 0) {
#pragma unroll
      for (int n = 0; n < 24; ++n) acc[n] *= alpha;
    }
    float pb[8];
    {
      const float a0 = dpp<0x00>(p), a1 = dpp<0x55>(p), a2 = dpp<0xAA>(p), a3 = dpp<0xFF>(p);
      pb[0] = dpp_upd<HMIR, 0xA>(a0, a0); pb[4] = dpp_upd<HMIR, 0x5>(a0, a0);
      pb[1] = dpp_upd<HMIR, 0xA>(a1, a1); pb[5] = dpp_upd<HMIR, 0x5>(a1, a1);
      pb[2] = dpp_upd<HMIR, 0xA>(a2, a2); pb[6] = dpp_upd<HMIR, 0x5>(a2, a2);
      pb[3] = dpp_upd<HMIR, 0xA>(a3, a3); pb[7] = dpp_upd<HMIR, 0x5>(a3, a3);
    }
    pv<BASE + 24, 0>(ring, s, tl, acc, pb);
    blocks<BLK + 1>(ring, s, tl, acc, qf, m, l, c);
  }
}

__device__ __forceinline__ u32x4 ld_g2(rsrc_t r, u32 off) { return __builtin_amdgcn_raw_buffer_load_b128(r, off, 0, AUX_SC1); }
__device__ __forceinline__ void st_g2(rsrc_t r, u32 off, u32x4 v) { __builtin_amdgcn_raw_buffer_store_b128(v, r, off, 0, AUX_SC1); }
__device__ __forceinline__ bool tags_ok(const u32x4 v) { return v[1] == TAG && v[3] == TAG; }
#define POLL_FENCE() asm volatile("" ::: "memory")
#define STAMP(i) do { } while (0)

__global__ void __launch_bounds__(512, 2)
fused_kernel(const u16* __restrict__ x, const u16* __restrict__ kc, const u16* __restrict__ vc,
             const u16* __restrict__ wq, const u16* __restrict__ wo, const u16* __restrict__ w1, const u16* __restrict__ b1,
             const u16* __restrict__ w2, const u16* __restrict__ b2, unsigned char* __restrict__ ws, u16* __restrict__ out) {
  __shared__ __attribute__((aligned(16))) float s_red[WAVES][DH];
  __shared__ __attribute__((aligned(16))) float s_h[2][DM];
  __shared__ __attribute__((aligned(16))) float s_y[WAVES][40];
  __shared__ __attribute__((aligned(16))) u16 s_q[DH];
  __shared__ float s_m[WAVES], s_l[WAVES];

  const int bid = blockIdx.x;
  const int g = bid & 7, jj = bid >> 3;
  const int h = g * 4 + (jj >> 3);
  const int b = (jj >> 2) & 1, sp = jj & 3;
  const int tid = threadIdx.x, lane = tid & 63;
  const int wave = __builtin_amdgcn_readfirstlane(tid >> 6);
  const int r = lane >> 3, c = lane & 7;
  const bool role1 = jj < 12;

  STAMP(0);
  const rsrc_t rws = __builtin_amdgcn_make_buffer_rsrc((void*)ws, 0, WS_BYTES, 0x00020000);
  const u32 my_h1 = OFF_H1 + (u32)(g * 32 + jj) * H1_STRIDE;
  if (wave == 7) {
    const u32x4 z4 = {0u, 0u, 0u, 0u};
    st_g2(rws, my_h1 + lane * 16, z4);
    if (lane < 40) st_g2(rws, my_h1 + 1024 + lane * 16, z4);
    if (lane < 20) st_g2(rws, OFF_H2 + (u32)((g * 2 + lane / 10) * DM + 20 * jj + 2 * (lane % 10)) * 8, z4);
    if (role1 && lane < 8) st_g2(rws, OFF_H3 + (u32)(192 * g + 16 * jj + 2 * lane) * 8, z4);
  }
  const u16* xb = x + b * DM;
  u32 xs[5];
#pragma unroll
  for (int i = 0; i < 5; ++i) xs[i] = reinterpret_cast<const u32*>(xb)[lane + 64 * i];
  u32 xr[10];
#pragma unroll
  for (int t = 0; t < 10; ++t) xr[t] = xb[80 * wave + 8 * t + r];

  Streams s;
  Tail tl;
  {
    const u16* wqp = wq + (size_t)(80 * wave) * HD + h * DH;
    const size_t kvoff = ((size_t)(b * NH + h) * SK + sp * KEYS_WG + wave * KEYS_WAVE) * DH;
    s.rw = __builtin_amdgcn_make_buffer_rsrc((void*)wqp, 0, (79 * HD + DH) * 2, 0x00020000);
    s.rk = __builtin_amdgcn_make_buffer_rsrc((void*)(kc + kvoff), 0, KEYS_WAVE * DH * 2, 0x00020000);
    s.rv = __builtin_amdgcn_make_buffer_rsrc((void*)(vc + kvoff), 0, KEYS_WAVE * DH * 2, 0x00020000);
    s.vo_w = r * HD * 2 + c * 16;
    s.vo_kv = r * DH * 2 + c * 16;
    tl.rwo = __builtin_amdgcn_make_buffer_rsrc((void*)(wo + (size_t)(768 * g + 96 * wave) * DM), 0, 96 * DM * 2, 0x00020000);
    tl.vo_wo = (c < 5) ? (r * DM + 20 * jj + 4 * c) * 2 : 0x40000000;
    tl.role1 = role1;
    tl.fp = reinterpret_cast<const u64*>(ws + OFF_FLAGS) + (tid & 255);
    tl.rw1 = __builtin_amdgcn_make_buffer_rsrc((void*)w1, 0, DM * FF * 2, 0x00020000);
    tl.vo_1 = role1 ? ((32 * wave + (lane & 31)) * FF + 192 * g + 16 * jj + 8 * (lane >> 5)) * 2 : 0x40000000;
    tl.rw2 = __builtin_amdgcn_make_buffer_rsrc((void*)w2, 0, FF * DM * 2, 0x00020000);
    tl.vo_2 = role1 ? 0x40000000 : (tid * DM + 80 * g + 4 * (jj - 12)) * 2;
  }
  u32x4 ring[RING];
  __builtin_amdgcn_sched_barrier(0);
  prologue<0, RING>(ring, s);
  const u64 epoch = __hip_atomic_load(reinterpret_cast<u64*>(ws + OFF_EPOCH), __ATOMIC_RELAXED, __HIP_MEMORY_SCOPE_AGENT);
  __builtin_amdgcn_sched_barrier(0);

  float xn[10];
  {
    float sum = 0.f;
#pragma unroll
    for (int i = 0; i < 5; ++i) sum += bflo(xs[i]) + bfhi(xs[i]);
    sum = wave_sum(sum);
    const float mean = sum * (1.0f / DM);
    float sq = 0.f;
#pragma unroll
    for (int i = 0; i < 5; ++i) { float a = bflo(xs[i]) - mean, bb = bfhi(xs[i]) - mean; sq += a * a + bb * bb; }
    sq = wave_sum(sq);
    const float mb = rbf(mean), vb = rbf(sq * (1.0f / DM));
    const float den = rbf(sqrtf(rbf(vb + 1e-5f)));
#pragma unroll
    for (int t = 0; t < 10; ++t) xn[t] = rbf(rbf(bf2f(xr[t]) - mb) / den);
  }
  float acc[24];
#pragma unroll
  for (int n = 0; n < 24; ++n) acc[n] = 0.f;
  qproj<0>(ring, s, tl, acc, xn);

  const u64 tag64 = epoch + 1ull;
  asm volatile("s_waitcnt vmcnt(%0)" ::"n"(RING) : "memory");
  __builtin_amdgcn_raw_buffer_store_b64(u32x2{(u32)tag64, (u32)(tag64 >> 32)}, rws, (tid == 448) ? OFF_FLAGS + bid * 8 : 0x40000000u, 0, AUX_SC1);
  __builtin_amdgcn_sched_barrier(0);

  const int n0 = 12 * (lane >> 5) + 6 * ((lane >> 4) & 1) + 3 * ((lane >> 3) & 1);
  {
    float o3[3];
    tr_reduce24(acc, o3);
#pragma unroll
    for (int v = 0; v < 3; ++v) { const int n = n0 + v; s_red[wave][(n >> 3) * 64 + c * 8 + (n & 7)] = o3[v]; }
  }
  __syncthreads();
  if (tid < DH) {
    float q = 0.f;
#pragma unroll
    for (int w = 0; w < WAVES; ++w) q += s_red[w][tid];
    s_q[tid] = (u16)f2bf(q);
  }
  __syncthreads();
  u32x4 qf[3];
#pragma unroll
  for (int j = 0; j < 3; ++j) qf[j] = *reinterpret_cast<const u32x4*>(&s_q[j * 64 + c * 8]);

  STAMP(1);
  float m = -INFINITY, l = 0.f;
#pragma unroll
  for (int n = 0; n < 24; ++n) acc[n] = 0.f;
  blocks<0>(ring, s, tl, acc, qf, m, l, c);

  STAMP(2);
  l = wave_sum(l);
  {
    float o3[3];
    tr_reduce24(acc, o3);
#pragma unroll
    for (int v = 0; v < 3; ++v) { const int n = n0 + v; s_red[wave][(n >> 3) * 64 + c * 8 + (n & 7)] = o3[v]; }
    if (lane == 0) { s_m[wave] = m; s_l[wave] = l; }
  }
  if (!__all(tl.flag == tag64)) {
    for (int spin = 0; spin < SPIN_MAX; ++spin) {
      const u64 f = __hip_atomic_load(tl.fp, __ATOMIC_RELAXED, __HIP_MEMORY_SCOPE_AGENT);
      if (__all(f == tag64)) break;
      __builtin_amdgcn_s_sleep(4);
    }
  }
  __syncthreads();
  if (tid <= 96) {
    float M = s_m[0];
#pragma unroll
    for (int w = 1; w < WAVES; ++w) M = fmaxf(M, s_m[w]);
    float L = 0.f, o0 = 0.f, o1 = 0.f;
    const int d = (tid < 96) ? 2 * tid : 0;
#pragma unroll
    for (int w = 0; w < WAVES; ++w) {
      const float e = __builtin_amdgcn_exp2f((s_m[w] - M) * LOG2E);
      const f32x2 a = *reinterpret_cast<const f32x2*>(&s_red[w][d]);
      L = fmaf(e, s_l[w], L);
      o0 = fmaf(e, a[0], o0);
      o1 = fmaf(e, a[1], o1);
    }
    if (tid == 96) { o0 = M; o1 = L; }
    st_g2(rws, my_h1 + tid * 16, u32x4{fbits(o0), TAG, fbits(o1), TAG});
  }
  __syncthreads();
  STAMP(3);

  float* s_o = &s_red[0][0];
  {
    u32x4 pa[4], pb2[4];
    const u32 base = OFF_H1 + (u32)(g * 32 + wave * 4) * H1_STRIDE;
    const u32 oa = lane * 16, ob = (lane < 33) ? 1024 + lane * 16 : 0x40000000u;
    unsigned done = 0;
    for (int spin = 0; spin < SPIN_MAX; ++spin) {
      POLL_FENCE();
#pragma unroll
      for (int q = 0; q < 4; ++q) {
        if (!(done & (1u << q))) {
          pa[q] = ld_g2(rws, base + q * H1_STRIDE + oa);
          pb2[q] = ld_g2(rws, (lane < 33) ? base + q * H1_STRIDE + ob : ob);
        }
      }
#pragma unroll
      for (int q = 0; q < 4; ++q) {
        const bool ok = tags_ok(pa[q]) && (lane >= 33 || tags_ok(pb2[q]));
        if (__all(ok)) done |= 1u << q;
      }
      if (done == 15u) break;
      __builtin_amdgcn_s_sleep(2);
    }
    float ms[4], ls[4];
#pragma unroll
    for (int q = 0; q < 4; ++q) {
      ms[q] = bitsf(__builtin_amdgcn_readlane(pb2[q][0], 32));
      ls[q] = bitsf(__builtin_amdgcn_readlane(pb2[q][2], 32));
    }
    const float M = fmaxf(fmaxf(ms[0], ms[1]), fmaxf(ms[2], ms[3]));
    float L = 0.f, a0 = 0.f, a1 = 0.f, b0 = 0.f, b1v = 0.f;
#pragma unroll
    for (int q = 0; q < 4; ++q) {
      const float e = __builtin_amdgcn_exp2f((ms[q] - M) * LOG2E);
      L = fmaf(e, ls[q], L);
      a0 = fmaf(e, bitsf(pa[q][0]), a0); a1 = fmaf(e, bitsf(pa[q][2]), a1);
      b0 = fmaf(e, bitsf(pb2[q][0]), b0); b1v = fmaf(e, bitsf(pb2[q][2]), b1v);
    }
    const int rowb = (wave >> 1) * DH, bb = wave & 1;
    s_o[(rowb + 2 * lane) * 2 + bb] = rbf(a0 / L);
    s_o[(rowb + 2 * lane + 1) * 2 + bb] = rbf(a1 / L);
    if (lane < 32) {
      s_o[(rowb + 128 + 2 * lane) * 2 + bb] = rbf(b0 / L);
      s_o[(rowb + 128 + 2 * lane + 1) * 2 + bb] = rbf(b1v / L);
    }
  }
  __syncthreads();
  STAMP(4);
  {
    float y[8];
#pragma unroll
    for (int e = 0; e < 8; ++e) y[e] = 0.f;
#pragma unroll
    for (int i = 0; i < 12; ++i) {
      const f32x2 o = *reinterpret_cast<const f32x2*>(&s_o[(96 * wave + 8 * i + r) * 2]);
      const float w0 = bflo(tl.wo[i][0]), w1v = bfhi(tl.wo[i][0]), w2v = bflo(tl.wo[i][1]), w3 = bfhi(tl.wo[i][1]);
      y[0] = fmaf(o[0], w0, y[0]); y[1] = fmaf(o[0], w1v, y[1]); y[2] = fmaf(o[0], w2v, y[2]); y[3] = fmaf(o[0], w3, y[3]);
      y[4] = fmaf(o[1], w0, y[4]); y[5] = fmaf(o[1], w1v, y[5]); y[6] = fmaf(o[1], w2v, y[6]); y[7] = fmaf(o[1], w3, y[7]);
    }
    float u[4], v[2];
#pragma unroll
    for (int e = 0; e < 4; ++e) u[e] = red32(y[e], y[e + 4]);
#pragma unroll
    for (int e = 0; e < 2; ++e) v[e] = red16(u[e], u[e + 2]);
    const float t = red8(v[0], v[1]);
    if (c < 5) s_y[wave][(lane >> 5) * 20 + 4 * c + 2 * ((lane >> 4) & 1) + ((lane >> 3) & 1)] = t;
  }
  __syncthreads();
  if (tid < 20) {
    float y0 = 0.f, y1 = 0.f;
#pragma unroll
    for (int w = 0; w < WAVES; ++w) { const f32x2 a = *reinterpret_cast<const f32x2*>(&s_y[w][2 * tid]); y0 += a[0]; y1 += a[1]; }
    st_g2(rws, OFF_H2 + (u32)((g * 2 + tid / 10) * DM + 20 * jj + 2 * (tid % 10)) * 8, u32x4{fbits(y0), TAG, fbits(y1), TAG});
  }

  STAMP(5);
  if (role1) {
    const u32 xa = reinterpret_cast<const u32*>(x)[tid];
    const u32 xbv = (tid < 128) ? reinterpret_cast<const u32*>(x)[512 + tid] : 0u;
    const float bias0 = bf2f(b1[192 * g + 16 * jj + (tid & 15)]);
    {
      u32x4 pa[8], pb2[8];
      const u32 oa = OFF_H2 + tid * 16, ob = (tid < 128) ? OFF_H2 + (512 + tid) * 16 : 0x40000000u;
      for (int spin = 0; spin < SPIN_MAX; ++spin) {
        POLL_FENCE();
#pragma unroll
        for (int q = 0; q < 8; ++q) pa[q] = ld_g2(rws, oa + q * (2 * DM * 8));
        if (wave < 2) {
#pragma unroll
          for (int q = 0; q < 8; ++q) pb2[q] = ld_g2(rws, ob + q * (2 * DM * 8));
        }
        bool ok = true;
#pragma unroll
        for (int q = 0; q < 8; ++q) ok = ok && tags_ok(pa[q]);
        if (wave < 2) {
#pragma unroll
          for (int q = 0; q < 8; ++q) ok = ok && tags_ok(pb2[q]);
        }
        if (__all(ok)) break;
        __builtin_amdgcn_s_sleep(2);
      }
      float y0 = 0.f, y1 = 0.f;
#pragma unroll
      for (int q = 0; q < 8; ++q) { y0 += bitsf(pa[q][0]); y1 += bitsf(pa[q][2]); }
      float* sh = &s_h[0][0];
      *reinterpret_cast<f32x2*>(&sh[2 * tid]) = f32x2{rbf(bflo(xa) + rbf(y0)), rbf(bfhi(xa) + rbf(y1))};
      if (wave < 2) {
        y0 = 0.f; y1 = 0.f;
#pragma unroll
        for (int q = 0; q < 8; ++q) { y0 += bitsf(pb2[q][0]); y1 += bitsf(pb2[q][2]); }
        *reinterpret_cast<f32x2*>(&sh[1024 + 2 * tid]) = f32x2{rbf(bflo(xbv) + rbf(y0)), rbf(bfhi(xbv) + rbf(y1))};
      }
    }
    __syncthreads();
    STAMP(6);
    float mb[2], den[2];
#pragma unroll
    for (int bb = 0; bb < 2; ++bb) {
      f32x2 hv[5];
#pragma unroll
      for (int i = 0; i < 5; ++i) hv[i] = *reinterpret_cast<const f32x2*>(&s_h[bb][2 * (lane + 64 * i)]);
      float sum = 0.f;
#pragma unroll
      for (int i = 0; i < 5; ++i) sum += hv[i][0] + hv[i][1];
      const float mean = wave_sum(sum) * (1.0f / DM);
      float sq = 0.f;
#pragma unroll
      for (int i = 0; i < 5; ++i) { const float a = hv[i][0] - mean, e = hv[i][1] - mean; sq += a * a + e * e; }
      sq = wave_sum(sq);
      mb[bb] = rbf(mean);
      den[bb] = rbf(sqrtf(rbf(rbf(sq * (1.0f / DM)) + 1e-5f)));
    }
    float a[16];
#pragma unroll
    for (int e = 0; e < 16; ++e) a[e] = 0.f;
#pragma unroll
    for (int i = 0; i < 3; ++i) {
      const int row = 32 * wave + (lane & 31) + 256 * i;
      const int rc = row < DM ? row : 0;
      const float n0v = rbf(rbf(s_h[0][rc] - mb[0]) / den[0]);
      const float n1v = rbf(rbf(s_h[1][rc] - mb[1]) / den[1]);
      fma8(a, tl.w[i], n0v);
      fma8(a + 8, tl.w[i], n1v);
    }
    float u[8], v[4], w2r[2];
#pragma unroll
    for (int e = 0; e < 8; ++e) u[e] = red16(a[e], a[e + 8]);
#pragma unroll
    for (int e = 0; e < 4; ++e) v[e] = red8(u[e], u[e + 4]);
#pragma unroll
    for (int e = 0; e < 2; ++e) w2r[e] = red4m(v[e], v[e + 2]);
    float t = red2(w2r[0], w2r[1], lane);
    t += dpp<XOR1>(t);
    float* s_p = &s_y[0][0];
    if ((lane & 1) == 0) s_p[wave * 32 + ((lane >> 4) & 1) * 16 + (lane >> 5) * 8 + ((lane >> 3) & 1) * 4 + ((lane >> 2) & 1) * 2 + ((lane >> 1) & 1)] = t;
    __syncthreads();
    if (tid < 32) {
      float sacc = 0.f;
#pragma unroll
      for (int w = 0; w < WAVES; ++w) sacc += s_p[w * 32 + tid];
      const float hh = rbf(rbf(sacc) + bias0);
      const float rr = hh > 0.f ? hh : 0.f;
      const u32 hb = f2bf(rr * rr);
      const u32 other = __shfl_xor(hb, 16);
      const u32 pk = hb | (other << 16);
      const u32 pk1 = __shfl_xor(pk, 1);
      if (tid < 16 && (tid & 1) == 0) st_g2(rws, OFF_H3 + (u32)(192 * g + 16 * jj + tid) * 8, u32x4{pk, TAG, pk1, TAG});
    }
    STAMP(7);
  } else {
    const int col0 = 80 * g + 4 * (jj - 12);
    float hres = 0.f, bias = 0.f;
    if (wave == 0) {
      const int q = lane >> 2, bb = (lane >> 1) & 1, pr = lane & 1;
      const u32 off = (lane < 32) ? OFF_H2 + (u32)((q * 2 + bb) * DM + col0 + 2 * pr) * 8 : 0x40000000u;
      const u32 xw = reinterpret_cast<const u32*>(x)[(bb * DM + col0 + 2 * pr) >> 1];
      u32x4 pv4;
      for (int spin = 0; spin < SPIN_MAX; ++spin) {
        POLL_FENCE();
        pv4 = ld_g2(rws, off);
        if (__all(lane >= 32 || tags_ok(pv4))) break;
        __builtin_amdgcn_s_sleep(2);
      }
      float y0 = 0.f, y1 = 0.f;
#pragma unroll
      for (int qq = 0; qq < 8; ++qq) {
        y0 += bitsf(__shfl(pv4[0], qq * 4 + (lane & 3)));
        y1 += bitsf(__shfl(pv4[2], qq * 4 + (lane & 3)));
      }
      const float h0 = rbf(bflo(xw) + rbf(y0)), h1 = rbf(bfhi(xw) + rbf(y1));
      const int src = ((lane >> 2) & 1) * 2 + ((lane >> 1) & 1);
      const float sh0 = __shfl(h0, src), sh1 = __shfl(h1, src);
      hres = (lane & 1) ? sh1 : sh0;
      bias = bf2f(b2[col0 + (lane & 3)]);
    }
    STAMP(6);
    u64 hv[3];
    const u64* hp = reinterpret_cast<const u64*>(ws + OFF_H3) + tid;
    for (int spin = 0; spin < SPIN_MAX; ++spin) {
      bool ok = true;
#pragma unroll
      for (int i = 0; i < 3; ++i) { hv[i] = __hip_atomic_load(hp + 512 * i, __ATOMIC_RELAXED, __HIP_MEMORY_SCOPE_AGENT); ok = ok && ((u32)(hv[i] >> 32) == TAG); }
      if (__all(ok)) break;
      __builtin_amdgcn_s_sleep(2);
    }
    STAMP(7);
    float y[8];
#pragma unroll
    for (int e = 0; e < 8; ++e) y[e] = 0.f;
#pragma unroll
    for (int i = 0; i < 3; ++i) {
      const float h0 = bflo((u32)hv[i]), h1 = bfhi((u32)hv[i]);
      const float w0 = bflo(tl.w2[i][0]), w1v = bfhi(tl.w2[i][0]), w2v = bflo(tl.w2[i][1]), w3 = bfhi(tl.w2[i][1]);
      y[0] = fmaf(h0, w0, y[0]); y[1] = fmaf(h0, w1v, y[1]); y[2] = fmaf(h0, w2v, y[2]); y[3] = fmaf(h0, w3, y[3]);
      y[4] = fmaf(h1, w0, y[4]); y[5] = fmaf(h1, w1v, y[5]); y[6] = fmaf(h1, w2v, y[6]); y[7] = fmaf(h1, w3, y[7]);
    }
    float u[4], v[2];
#pragma unroll
    for (int e = 0; e < 4; ++e) u[e] = red32(y[e], y[e + 4]);
#pragma unroll
    for (int e = 0; e < 2; ++e) v[e] = red16(u[e], u[e + 2]);
    float t = red8(v[0], v[1]);
    t += dpp<HMIR>(t);
    t += dpp<XOR2>(t);
    t += dpp<XOR1>(t);
    __syncthreads();
    float* s_p = &s_y[0][0];
    if (c == 0) s_p[wave * 8 + r] = t;
    __syncthreads();
    if (tid < 8) {
      float sacc = 0.f;
#pragma unroll
      for (int w = 0; w < WAVES; ++w) sacc += s_p[w * 8 + tid];
      const float o = rbf(rbf(sacc) + bias);
      out[(tid >> 2) * DM + col0 + (tid & 3)] = (u16)f2bf(hres + o);
    }
    STAMP(8);
    if (bid == 255 && tid == 0) __hip_atomic_store(reinterpret_cast<u64*>(ws + OFF_EPOCH), tag64, __ATOMIC_RELAXED, __HIP_MEMORY_SCOPE_AGENT);
  }
}

__global__ void __launch_bounds__(256) touch_k(const char* __restrict__ k, const char* __restrict__ v, unsigned char* ws, unsigned n, unsigned stride) {
  unsigned i = blockIdx.x * 256 + threadIdx.x;
  unsigned acc = 0;
  if (i < n) {
    acc = __builtin_nontemporal_load(reinterpret_cast<const unsigned*>(k + (size_t)i * stride)) ^ __builtin_nontemporal_load(reinterpret_cast<const unsigned*>(v + (size_t)i * stride));
  }
  if (acc == 0x12345679u) ws[(4u << 20) + (i & 1023)] = 1;
}
extern "C" void kernel_launch(void* const* d_in, const int* in_sizes, int n_in,
                              void* d_out, int out_size, void* d_ws, size_t ws_size,
                              hipStream_t stream) {
  touch_k<<<192, 256, 0, stream>>>((const char*)d_in[1], (const char*)d_in[2], (unsigned char*)d_ws, 49152u, 4096u);
  fused_kernel<<<256, 512, 0, stream>>>((const u16*)d_in[0], (const u16*)d_in[1], (const u16*)d_in[2], (const u16*)d_in[3],
                                        (const u16*)d_in[4], (const u16*)d_in[6], (const u16*)d_in[7], (const u16*)d_in[8],
                                        (const u16*)d_in[9], (unsigned char*)d_ws, (u16*)d_out);
}
```

```cpp
#include <hip/hip_runtime.h>
#include <hip/hip_bf16.h>
#include <stdint.h>

typedef unsigned int u32;
typedef unsigned short u16;
typedef unsigned long long u64;
using u32x4 = __attribute__((ext_vector_type(4))) unsigned;
using u32x2 = __attribute__((ext_vector_type(2))) unsigned;
using f32x2 = __attribute__((ext_vector_type(2))) float;
using f32x4 = __attribute__((ext_vector_type(4))) float;
using bf16x2v = __attribute__((ext_vector_type(2))) __bf16;
typedef __amdgpu_buffer_rsrc_t rsrc_t;

constexpr int DM = 640, NH = 32, SK = 8192, DH = 192, FF = 1536;
constexpr int HD = NH * DH;
constexpr int WAVES = 8;
constexpr float SCALE = 0.07216878364870322f;
constexpr float LOG2E = 1.4426950408889634f;

constexpr int RING = 12;
constexpr u32 NU = SK / 16;
#define PL 14
constexpr u32 NLOC = 8 * PL, NSH = 32 * (16 - PL);
#define PRO_A 4

constexpr u32 OFF_EPOCH = 0;
constexpr u32 OFF_FLAGS = 1024;
constexpr u32 OFF_CNT = 4096;
constexpr u32 OFF_H0 = 12288;
constexpr u32 OFF_H1 = OFF_H0 + 256 * 3072;
constexpr u32 OFF_H2 = OFF_H1 + 256 * 1024;
constexpr u32 OFF_H3 = OFF_H2 + 8 * 2 * DM * 4;
constexpr u32 WS_BYTES = OFF_H3 + 48 * 2 * DM * 4;
constexpr u32 OOB = 0x40000000u;
constexpr int SPIN_MAX = 1 << 18;
constexpr int AUX_NT = 2, AUX_SC1 = 16;

__device__ __forceinline__ float bf2f(u32 h) { return __builtin_bit_cast(float, h << 16); }
__device__ __forceinline__ float bflo(u32 w) { return __builtin_bit_cast(float, w << 16); }
__device__ __forceinline__ float bfhi(u32 w) { return __builtin_bit_cast(float, w & 0xffff0000u); }
__device__ __forceinline__ float rbf(float x) { return __bfloat162float(__float2bfloat16(x)); }
__device__ __forceinline__ u32 f2bf(float x) { return (u32)__builtin_bit_cast(u16, __float2bfloat16(x)); }
__device__ __forceinline__ u32 fbits(float x) { return __builtin_bit_cast(u32, x); }
__device__ __forceinline__ float bitsf(u32 x) { return __builtin_bit_cast(float, x); }

__device__ __forceinline__ float dot2(u32 k, u32 q, float acc) {
  return __builtin_amdgcn_fdot2_f32_bf16(__builtin_bit_cast(bf16x2v, k), __builtin_bit_cast(bf16x2v, q), acc, false);
}
__device__ __forceinline__ u32 pack_lo(u32 a, u32 b) { return __builtin_amdgcn_perm(b, a, 0x05040100u); }
__device__ __forceinline__ u32 pack_hi(u32 a, u32 b) { return __builtin_amdgcn_perm(b, a, 0x07060302u); }

template <int CTRL> __device__ __forceinline__ float dpp(float x) {
  return __builtin_bit_cast(float, __builtin_amdgcn_mov_dpp(__builtin_bit_cast(int, x), CTRL, 0xf, 0xf, true));
}
template <int CTRL, int BANK> __device__ __forceinline__ float dpp_upd(float old, float x) {
  return __builtin_bit_cast(float, __builtin_amdgcn_update_dpp(__builtin_bit_cast(int, old), __builtin_bit_cast(int, x), CTRL, 0xf, BANK, false));
}
constexpr int XOR1 = 0xB1, XOR2 = 0x4E, HMIR = 0x141, ROR8 = 0x128, ROR4 = 0x124, ROR12 = 0x12C, IDENT = 0xE4;

__device__ __forceinline__ float wave_max(float x) {
  x = fmaxf(x, dpp<XOR1>(x));
  x = fmaxf(x, dpp<XOR2>(x));
  x = fmaxf(x, dpp<HMIR>(x));
  x = fmaxf(x, dpp<ROR8>(x));
  auto s = __builtin_amdgcn_permlane16_swap(__float_as_uint(x), __float_as_uint(x), false, false);
  x = fmaxf(__uint_as_float(s[0]), __uint_as_float(s[1]));
  auto t = __builtin_amdgcn_permlane32_swap(__float_as_uint(x), __float_as_uint(x), false, false);
  return fmaxf(__uint_as_float(t[0]), __uint_as_float(t[1]));
}
__device__ __forceinline__ float wave_sum(float x) {
  x += dpp<XOR1>(x);
  x += dpp<XOR2>(x);
  x += dpp<HMIR>(x);
  x += dpp<ROR8>(x);
  auto s = __builtin_amdgcn_permlane16_swap(__float_as_uint(x), __float_as_uint(x), false, false);
  x = __uint_as_float(s[0]) + __uint_as_float(s[1]);
  auto t = __builtin_amdgcn_permlane32_swap(__float_as_uint(x), __float_as_uint(x), false, false);
  return __uint_as_float(t[0]) + __uint_as_float(t[1]);
}
__device__ __forceinline__ float red32(float a, float b) {
  auto s = __builtin_amdgcn_permlane32_swap(__float_as_uint(a), __float_as_uint(b), false, false);
  return __uint_as_float(s[0]) + __uint_as_float(s[1]);
}
__device__ __forceinline__ float red16(float a, float b) {
  auto s = __builtin_amdgcn_permlane16_swap(__float_as_uint(a), __float_as_uint(b), false, false);
  return __uint_as_float(s[0]) + __uint_as_float(s[1]);
}
__device__ __forceinline__ float red8(float a, float b) { return dpp_upd<ROR8, 0xC>(a, b) + dpp_upd<ROR8, 0x3>(b, a); }
__device__ __forceinline__ float red4m(float a, float b) { return dpp_upd<HMIR, 0xA>(a, b) + dpp_upd<HMIR, 0x5>(b, a); }
__device__ __forceinline__ float red4(float a, float b) {
  return dpp_upd<IDENT, 0xA>(a, b) + dpp_upd<ROR4, 0xA>(dpp_upd<ROR12, 0x5>(a, a), b);
}
__device__ __forceinline__ float red2(float a, float b, int lane) { const float t = a + dpp<XOR2>(a), u = b + dpp<XOR2>(b); return (lane & 2) ? u : t; }
__device__ __forceinline__ float red1(float a, float b, int lane) { const float t = a + dpp<XOR1>(a), u = b + dpp<XOR1>(b); return (lane & 1) ? u : t; }

__device__ __forceinline__ void tr_reduce24(float (&a)[24], float (&o)[3]) {
  float b[12], c[6];
#pragma unroll
  for (int v = 0; v < 12; ++v) b[v] = red32(a[v], a[v + 12]);
#pragma unroll
  for (int v = 0; v < 6; ++v) c[v] = red16(b[v], b[v + 6]);
#pragma unroll
  for (int v = 0; v < 3; ++v) o[v] = red8(c[v], c[v + 3]);
}
__device__ __forceinline__ rsrc_t mk_rsrc(const void* p, u32 bytes) { return __builtin_amdgcn_make_buffer_rsrc((void*)p, 0, bytes, 0x00020000); }

struct Blk { rsrc_t rk, rv; };

template <int N> __device__ __forceinline__ u32x4 issue(const Blk& b, const u32 (&vo)[2]) {
  constexpr int isv = N / 6, q = N % 6, i = q / 3, j = q % 3;
  if constexpr (isv) return __builtin_amdgcn_raw_buffer_load_b128(b.rv, vo[i], j * 128, AUX_NT);
  else return __builtin_amdgcn_raw_buffer_load_b128(b.rk, vo[i], j * 128, AUX_NT);
}
template <int N, int E> __device__ __forceinline__ void prologue(u32x4 (&ring)[RING], const Blk& b, const u32 (&vo)[2]) {
  if constexpr (N < E) { ring[N] = issue<N>(b, vo); __builtin_amdgcn_sched_barrier(0); prologue<N + 1, E>(ring, b, vo); }
}
__device__ __forceinline__ void fma8(float* acc, const u32x4 w, const float p) {
#pragma unroll
  for (int k = 0; k < 4; ++k) {
    acc[2 * k] = fmaf(p, bflo(w[k]), acc[2 * k]);
    acc[2 * k + 1] = fmaf(p, bfhi(w[k]), acc[2 * k + 1]);
  }
  asm volatile("" : "+v"(acc[0]), "+v"(acc[1]), "+v"(acc[2]), "+v"(acc[3]), "+v"(acc[4]), "+v"(acc[5]), "+v"(acc[6]), "+v"(acc[7]));
}
__device__ __forceinline__ void unit_blk(u32 u, const char* kb, const char* vb, Blk& o) {
  const u32 uc = u < NU ? u : 0u, nb = u < NU ? 6144u : 0u;
  o.rk = mk_rsrc(kb + (size_t)uc * 6144, nb);
  o.rv = mk_rsrc(vb + (size_t)uc * 6144, nb);
}
template <int N> __device__ __forceinline__ float kdot(u32x4 (&ring)[RING], const Blk& nx, const u32 (&vo)[2], const u32x4 q, float d) {
#pragma unroll
  for (int k = 0; k < 4; ++k) d = dot2(ring[N][k], q[k], d);
  asm volatile("" : "+v"(d));
  ring[N] = issue<N>(nx, vo);
  __builtin_amdgcn_sched_barrier(0);
  return d;
}
template <int N> __device__ __forceinline__ void vfma(u32x4 (&ring)[RING], const Blk& nx, const u32 (&vo)[2], float* acc, float p) {
  fma8(acc, ring[N], p);
  ring[N] = issue<N>(nx, vo);
  __builtin_amdgcn_sched_barrier(0);
}

__device__ __forceinline__ u32x4 ld_g(rsrc_t r, u32 off) { return __builtin_amdgcn_raw_buffer_load_b128(r, off, 0, AUX_SC1); }
__device__ __forceinline__ void st_g(rsrc_t r, u32 off, u32x4 v) { __builtin_amdgcn_raw_buffer_store_b128(v, r, off, 0, AUX_SC1); }
__device__ __forceinline__ u32 umin(u32 a, u32 b) { return a < b ? a : b; }
__device__ __forceinline__ u32 min4(const u32x4 v) { return umin(umin(v[0], v[1]), umin(v[2], v[3])); }
#define POLL_FENCE() asm volatile("" ::: "memory")
#define PIN(x) asm volatile("" : "+v"(x))
#define STAMP(i) do { } while (0)

__global__ void __launch_bounds__(512, 2)
fused_kernel(const u16* __restrict__ x, const u16* __restrict__ kc, const u16* __restrict__ vc,
             const u16* __restrict__ wq, const u16* __restrict__ wo, const u16* __restrict__ w1, const u16* __restrict__ b1,
             const u16* __restrict__ w2, const u16* __restrict__ b2, unsigned char* __restrict__ ws, u16* __restrict__ out) {
  __shared__ __attribute__((aligned(16))) float s_big[20 * 2 * DH];
  __shared__ __attribute__((aligned(16))) float s_y[WAVES][128];
  __shared__ __attribute__((aligned(16))) u32 s_xn[2][20][2];
  __shared__ __attribute__((aligned(16))) u32 s_hn[2][DM / 2];
  __shared__ __attribute__((aligned(16))) u16 s_a[2][32];
  __shared__ __attribute__((aligned(16))) u16 s_q[DH];
  __shared__ __attribute__((aligned(16))) float s_hres[32];
  __shared__ float s_m[WAVES], s_l[WAVES];
  __shared__ f32x2 s_st[WAVES];
  __shared__ u32 s_cnt;

  const int bid = blockIdx.x;
  const int h = bid >> 3, b = (bid >> 2) & 1, e8 = bid & 7;
  const int Q = bid >> 2, sq4 = bid & 3;
  const int tid = threadIdx.x, lane = tid & 63;
  const int wave = __builtin_amdgcn_readfirstlane(tid >> 6);
  const int r = lane >> 3, c = lane & 7;
  const bool roleO = (sq4 == 0) || (sq4 == 1 && Q < 16), roleM = (sq4 == 1 && Q >= 16), roleF = (sq4 == 2 && Q < 40);
  const int wi = (sq4 == 0) ? Q : 64 + Q;
  const int g = wi / 10, ct = wi % 10;
  const int mi = Q - 16;
  const int ub = (mi & ~3) | ((mi & 1) << 1) | ((mi >> 1) & 1);

  STAMP(0);
  const rsrc_t rws = mk_rsrc(ws, WS_BYTES);
  const u32 my_h1 = OFF_H1 + (u32)bid * 1024;
  const u32 cnt_off = OFF_CNT + (u32)Q * 128;
  {
    u32 zo = OOB;
    if (wave == 7) { if (lane < 49) zo = my_h1 + lane * 16; else if (lane == 63 && sq4 == 0) zo = cnt_off; }
    else if (wave == 6) { if (roleO && lane < 32) zo = OFF_H2 + (u32)((g * 2 + (lane >> 4)) * DM + 64 * ct + 4 * (lane & 15)) * 4; }
    else if (wave < 5) { if (roleM) zo = OFF_H3 + (u32)mi * (2 * DM * 4) + tid * 16; }
    st_g(rws, zo, u32x4{0u, 0u, 0u, 0u});
  }
  const u32x2 epoch = __builtin_amdgcn_raw_buffer_load_b64(rws, OFF_EPOCH, 0, AUX_SC1);
  const u16* xb = x + (wave & 1) * DM;
  u32 xs[5];
#pragma unroll
  for (int i = 0; i < 5; ++i) xs[i] = reinterpret_cast<const u32*>(xb)[lane + 64 * i];
  const int xrow = (wave >> 1) * 20 + (lane < 20 ? lane : 0);
  u32 xr = xb[80 * e8 + xrow];
  const int ci = tid % 24, rg = tid / 24;
  u32x4 wqv[4];
  {
    const rsrc_t rw = mk_rsrc(wq + (size_t)(80 * e8) * HD + h * DH, (79 * HD + DH) * 2);
    const u32 vo = (tid < 480) ? (u32)(rg * HD * 2 + ci * 16) : OOB;
#pragma unroll
    for (int i = 0; i < 4; ++i) wqv[i] = __builtin_amdgcn_raw_buffer_load_b128(rw, vo, i * 20 * HD * 2, AUX_NT);
  }
  __builtin_amdgcn_sched_barrier(0);

  u32x4 tw[13];
  auto issue_tail = [&]() {
    if (roleO) {
      const rsrc_t rwo = mk_rsrc(wo + (size_t)(768 * g + 96 * wave) * DM, 96 * DM * 2);
      const u32 vo = (u32)((12 * r * DM + 64 * ct + 8 * c) * 2);
#pragma unroll
      for (int i = 0; i < 12; ++i) tw[i] = __builtin_amdgcn_raw_buffer_load_b128(rwo, vo, i * DM * 2, AUX_NT);
      tw[12] = u32x4{0u, 0u, 0u, 0u};
    } else if (roleM) {
      const rsrc_t r1 = mk_rsrc(w1 + (size_t)(80 * wave) * FF, 80 * FF * 2);
      const u32 co = (u32)((32 * ub + 8 * (lane & 3)) * 2);
      const u32 vo = (u32)(2 * (lane >> 2) * FF * 2) + co;
      tw[0] = __builtin_amdgcn_raw_buffer_load_b128(r1, vo, 0, AUX_NT);
      tw[1] = __builtin_amdgcn_raw_buffer_load_b128(r1, vo, FF * 2, AUX_NT);
      tw[2] = __builtin_amdgcn_raw_buffer_load_b128(r1, vo, 32 * FF * 2, AUX_NT);
      tw[3] = __builtin_amdgcn_raw_buffer_load_b128(r1, vo, 33 * FF * 2, AUX_NT);
      tw[4] = __builtin_amdgcn_raw_buffer_load_b128(r1, (u32)((lane >> 2) * FF * 2) + co, 64 * FF * 2, AUX_NT);
      const rsrc_t r2 = mk_rsrc(w2 + (size_t)(32 * ub) * DM, 32 * DM * 2);
      const u32 v2 = (wave < 5) ? (u32)((8 * (lane >> 4) * DM + 8 * (16 * wave + (lane & 15))) * 2) : OOB;
#pragma unroll
      for (int k = 0; k < 8; ++k) tw[5 + k] = __builtin_amdgcn_raw_buffer_load_b128(r2, v2, k * DM * 2, AUX_NT);
    } else {
#pragma unroll
      for (int i = 0; i < 13; ++i) tw[i] = u32x4{0u, 0u, 0u, 0u};
    }
  };

  const char* kb = reinterpret_cast<const char*>(kc + (size_t)(b * NH + h) * SK * DH);
  const char* vb = reinterpret_cast<const char*>(vc + (size_t)(b * NH + h) * SK * DH);
  const u32 vo[2] = {(u32)(r * 384 + c * 16), (u32)(3072 + r * 384 + c * 16)};
  Blk cur, nxt;
  unit_blk((u32)(16 * (sq4 * 8 + wave)), kb, vb, cur);
  unit_blk((u32)(16 * (sq4 * 8 + wave) + 1), kb, vb, nxt);
  if (tid == 0) s_cnt = 24u;
  u32x4 ring[RING];
  __builtin_amdgcn_sched_barrier(0);
  __builtin_amdgcn_s_barrier();
  __builtin_amdgcn_sched_barrier(0);
  prologue<0, PRO_A>(ring, cur, vo);
  __builtin_amdgcn_sched_barrier(0);
  PIN(xr);

  {
    float sum = 0.f;
#pragma unroll
    for (int i = 0; i < 5; ++i) sum += bflo(xs[i]) + bfhi(xs[i]);
    sum = wave_sum(sum);
    const float mean = sum * (1.0f / DM);
    float sq = 0.f;
#pragma unroll
    for (int i = 0; i < 5; ++i) { float a = bflo(xs[i]) - mean, bb = bfhi(xs[i]) - mean; sq += a * a + bb * bb; }
    sq = wave_sum(sq);
    const float mb = rbf(mean), vb = rbf(sq * (1.0f / DM));
    const float den = rbf(sqrtf(rbf(vb + 1e-5f)));
    const u32 xn = f2bf(rbf(bf2f(xr) - mb) / den);
    if (lane < 20) reinterpret_cast<u16*>(&s_xn[wave & 1][lane][0])[wave >> 1] = (u16)xn;
  }
  __syncthreads();
  STAMP(1);
  const u32 tagA = epoch[0] + 1u, tagB = epoch[1] + 1u;
  {
    const int rgc = rg < 20 ? rg : 0;
    const u32x2 xa = *reinterpret_cast<const u32x2*>(&s_xn[0][rgc][0]);
    const u32x2 xc = *reinterpret_cast<const u32x2*>(&s_xn[1][rgc][0]);
    float a0[8], a1[8];
#pragma unroll
    for (int k = 0; k < 4; ++k) {
      const u32 p01l = pack_lo(wqv[0][k], wqv[1][k]), p01h = pack_hi(wqv[0][k], wqv[1][k]);
      const u32 p23l = pack_lo(wqv[2][k], wqv[3][k]), p23h = pack_hi(wqv[2][k], wqv[3][k]);
      a0[2 * k] = dot2(p23l, xa[1], dot2(p01l, xa[0], 0.f));
      a0[2 * k + 1] = dot2(p23h, xa[1], dot2(p01h, xa[0], 0.f));
      a1[2 * k] = dot2(p23l, xc[1], dot2(p01l, xc[0], 0.f));
      a1[2 * k + 1] = dot2(p23h, xc[1], dot2(p01h, xc[0], 0.f));
    }
    if (tid < 480) {
      float* d0 = &s_big[(rg * 2 + 0) * DH + ci * 8];
      float* d1 = &s_big[(rg * 2 + 1) * DH + ci * 8];
      *reinterpret_cast<f32x4*>(d0) = f32x4{a0[0], a0[1], a0[2], a0[3]};
      *reinterpret_cast<f32x4*>(d0 + 4) = f32x4{a0[4], a0[5], a0[6], a0[7]};
      *reinterpret_cast<f32x4*>(d1) = f32x4{a1[0], a1[1], a1[2], a1[3]};
      *reinterpret_cast<f32x4*>(d1 + 4) = f32x4{a1[4], a1[5], a1[6], a1[7]};
    }
  }
  __syncthreads();
  if (tid < 192) {
    float v0 = 0.f, v1 = 0.f;
#pragma unroll
    for (int k = 0; k < 20; ++k) { const f32x2 a = *reinterpret_cast<const f32x2*>(&s_big[k * 2 * DH + 2 * tid]); v0 += a[0]; v1 += a[1]; }
    st_g(rws, OFF_H0 + (u32)bid * 3072 + tid * 16, u32x4{fbits(v0), tagA, fbits(v1), tagB});
  } else if (tid == 448) {
    __builtin_amdgcn_raw_buffer_store_b64(u32x2{tagA, tagB}, rws, OFF_FLAGS + bid * 8, 0, AUX_SC1);
  }
  STAMP(2);
  __builtin_amdgcn_sched_barrier(0);
  __builtin_amdgcn_s_barrier();
  __builtin_amdgcn_sched_barrier(0);
  prologue<PRO_A, RING>(ring, cur, vo);
  __builtin_amdgcn_sched_barrier(0);
  __builtin_amdgcn_s_barrier();
  __builtin_amdgcn_sched_barrier(0);
  {
    const int gi = tid;
    const u32 off = (gi < 96) ? OFF_H0 + (u32)(h * 8) * 3072 + (u32)(b * 96 + gi) * 16 : OOB;
    u32x4 pq[8];
#pragma unroll
    for (int e = 0; e < 8; ++e) pq[e] = ld_g(rws, off + e * 3072);
    __builtin_amdgcn_sched_barrier(0);
    issue_tail();
    __builtin_amdgcn_sched_barrier(0);
    if (wave < 2) {
      for (int spin = 0; spin < SPIN_MAX; ++spin) {
        u32 bad = 0u;
#pragma unroll
        for (int e = 0; e < 8; ++e) bad |= (pq[e][1] ^ tagA) | (pq[e][3] ^ tagB);
        if (__all((bad == 0u) | (gi >= 96))) break;
        POLL_FENCE();
#pragma unroll
        for (int e = 0; e < 8; ++e) pq[e] = ld_g(rws, off + e * 3072);
      }
      float v0 = 0.f, v1 = 0.f;
#pragma unroll
      for (int e = 0; e < 8; ++e) { v0 += bitsf(pq[e][0]); v1 += bitsf(pq[e][2]); }
      if (gi < 96) reinterpret_cast<u32*>(s_q)[gi] = f2bf(v0) | (f2bf(v1) << 16);
    }
  }
  __syncthreads();
  u32x4 qf[3];
#pragma unroll
  for (int j = 0; j < 3; ++j) qf[j] = *reinterpret_cast<const u32x4*>(&s_q[j * 64 + c * 8]);
  __builtin_amdgcn_sched_barrier(0);

  STAMP(3);
  float m = -INFINITY, l = 0.f;
  float acc[24];
#pragma unroll
  for (int n = 0; n < 24; ++n) acc[n] = 0.f;
  const u32 vo_cnt = (lane == 0) ? cnt_off : OOB;
  bool nxt_ok = true;
  u32 iter = 0;
  u32 li = 16u + wave;
  for (;;) {
    const u32 tk = __builtin_amdgcn_raw_ptr_buffer_atomic_add_i32(1, rws, li >= NLOC ? vo_cnt : OOB, 0, AUX_SC1);
    u32 lk = 0u;
    if (lane == 0) lk = __hip_atomic_fetch_add(&s_cnt, 1u, __ATOMIC_RELAXED, __HIP_MEMORY_SCOPE_WORKGROUP);
    __builtin_amdgcn_sched_barrier(0);
    float d0 = kdot<0>(ring, nxt, vo, qf[0], 0.f);
    d0 = kdot<1>(ring, nxt, vo, qf[1], d0);
    d0 = kdot<2>(ring, nxt, vo, qf[2], d0);
    float d1 = kdot<3>(ring, nxt, vo, qf[0], 0.f);
    d1 = kdot<4>(ring, nxt, vo, qf[1], d1);
    d1 = kdot<5>(ring, nxt, vo, qf[2], d1);
    float sc = red4m(d0, d1);
    sc += dpp<XOR2>(sc);
    sc += dpp<XOR1>(sc);
    sc = rbf(rbf(sc) * SCALE);
    float bm = fmaxf(sc, dpp<HMIR>(sc));
    bm = fmaxf(bm, dpp<ROR8>(bm));
    {
      auto s1 = __builtin_amdgcn_permlane16_swap(__float_as_uint(bm), __float_as_uint(bm), false, false);
      bm = fmaxf(__uint_as_float(s1[0]), __uint_as_float(s1[1]));
      auto s2 = __builtin_amdgcn_permlane32_swap(__float_as_uint(bm), __float_as_uint(bm), false, false);
      bm = fmaxf(__uint_as_float(s2[0]), __uint_as_float(s2[1]));
    }
    const float mn = fmaxf(m, bm);
    const float alpha = __builtin_amdgcn_exp2f((m - mn) * LOG2E);
    const float p = __builtin_amdgcn_exp2f((sc - mn) * LOG2E);
    l = fmaf(l, alpha, p);
    m = mn;
#pragma unroll
    for (int n = 0; n < 24; ++n) acc[n] *= alpha;
    const float pb0 = dpp_upd<HMIR, 0xA>(p, p), pb1 = dpp_upd<HMIR, 0x5>(p, p);
    vfma<6>(ring, nxt, vo, acc + 0, pb0);
    vfma<7>(ring, nxt, vo, acc + 8, pb0);
    vfma<8>(ring, nxt, vo, acc + 16, pb0);
    vfma<9>(ring, nxt, vo, acc + 0, pb1);
    vfma<10>(ring, nxt, vo, acc + 8, pb1);
    vfma<11>(ring, nxt, vo, acc + 16, pb1);
    if (!nxt_ok) break;
    {
      const u32 t = __builtin_amdgcn_readfirstlane(tk);
      const u32 ul = (8u * sq4 + (li & 7u)) * 16u + (li >> 3);
      const u32 us = t < NSH ? (t & 31u) * 16u + PL + (t >> 5) : NU;
      const u32 u = li < NLOC ? ul : us;
      nxt_ok = u < NU;
      unit_blk(u, kb, vb, nxt);
      li = __builtin_amdgcn_readfirstlane(lk);
    }
  }
  const u32 vo_flag = (tid < 256) ? OFF_FLAGS + tid * 8 : OOB;
  u32x2 flag = __builtin_amdgcn_raw_buffer_load_b64(rws, vo_flag, 0, AUX_SC1);

  STAMP(4);
  float (*s_red)[DH] = reinterpret_cast<float (*)[DH]>(s_big);
  l = wave_sum(l) * 0.25f;
  {
    const int n0 = 12 * (lane >> 5) + 6 * ((lane >> 4) & 1) + 3 * ((lane >> 3) & 1);
    float o3[3];
    tr_reduce24(acc, o3);
#pragma unroll
    for (int v = 0; v < 3; ++v) { const int n = n0 + v; s_red[wave][(n >> 3) * 64 + c * 8 + (n & 7)] = o3[v]; }
    if (lane == 0) { s_m[wave] = m; s_l[wave] = l; }
  }
  __syncthreads();
  if (tid < 49) {
    float M = s_m[0];
#pragma unroll
    for (int w = 1; w < WAVES; ++w) M = fmaxf(M, s_m[w]);
    float L = 0.f;
    f32x4 o = {0.f, 0.f, 0.f, 0.f};
    const int d = (tid < 48) ? 4 * tid : 0;
#pragma unroll
    for (int w = 0; w < WAVES; ++w) {
      const float e = __builtin_amdgcn_exp2f((s_m[w] - M) * LOG2E);
      const f32x4 a = *reinterpret_cast<const f32x4*>(&s_red[w][d]);
      L = fmaf(e, s_l[w], L);
      o[0] = fmaf(e, a[0], o[0]); o[1] = fmaf(e, a[1], o[1]); o[2] = fmaf(e, a[2], o[2]); o[3] = fmaf(e, a[3], o[3]);
    }
    if (tid == 48) { o[0] = M; o[1] = L; }
    st_g(rws, my_h1 + tid * 16, u32x4{fbits(o[0]) | 1u, fbits(o[1]) | 1u, fbits(o[2]) | 1u, fbits(o[3]) | 1u});
  }
  STAMP(5);
  if (!(roleO || roleM || roleF)) return;
  if (!__all((tid >= 256) | (((flag[0] ^ tagA) | (flag[1] ^ tagB)) == 0u))) {
    for (int spin = 0; spin < SPIN_MAX; ++spin) {
      POLL_FENCE();
      flag = __builtin_amdgcn_raw_buffer_load_b64(rws, vo_flag, 0, AUX_SC1);
      if (__all((tid >= 256) | (((flag[0] ^ tagA) | (flag[1] ^ tagB)) == 0u))) break;
      __builtin_amdgcn_s_sleep(4);
    }
  }
  __syncthreads();

  if (roleO) {
    u32 wp[6][8];
#pragma unroll
    for (int i = 0; i < 6; ++i)
#pragma unroll
      for (int k = 0; k < 4; ++k) {
        wp[i][2 * k] = pack_lo(tw[2 * i][k], tw[2 * i + 1][k]); wp[i][2 * k + 1] = pack_hi(tw[2 * i][k], tw[2 * i + 1][k]);
        PIN(wp[i][2 * k]); PIN(wp[i][2 * k + 1]);
      }
    u32* s_o = reinterpret_cast<u32*>(s_big);
    {
      u32x4 pa[4];
      const u32 base = (lane < 49) ? OFF_H1 + (u32)(32 * g + 8 * (wave >> 1) + 4 * (wave & 1)) * 1024 + lane * 16 : OOB;
      for (int spin = 0; spin < SPIN_MAX; ++spin) {
        POLL_FENCE();
#pragma unroll
        for (int q = 0; q < 4; ++q) pa[q] = ld_g(rws, base + q * 1024);
        const u32 mn4 = umin(umin(min4(pa[0]), min4(pa[1])), umin(min4(pa[2]), min4(pa[3])));
        if (__all((mn4 != 0u) | (lane >= 49))) break;
        __builtin_amdgcn_s_sleep(1);
      }
      float ms[4], ls[4];
#pragma unroll
      for (int q = 0; q < 4; ++q) {
        ms[q] = bitsf(__builtin_amdgcn_readlane(pa[q][0], 48));
        ls[q] = bitsf(__builtin_amdgcn_readlane(pa[q][1], 48));
      }
      const float M = fmaxf(fmaxf(ms[0], ms[1]), fmaxf(ms[2], ms[3]));
      float L = 0.f, a[4] = {0.f, 0.f, 0.f, 0.f};
#pragma unroll
      for (int q = 0; q < 4; ++q) {
        const float e = __builtin_amdgcn_exp2f((ms[q] - M) * LOG2E);
        L = fmaf(e, ls[q], L);
#pragma unroll
        for (int k = 0; k < 4; ++k) a[k] = fmaf(e, bitsf(pa[q][k]), a[k]);
      }
      if (lane < 48)
        *reinterpret_cast<u32x2*>(&s_o[(wave & 1) * 384 + (wave >> 1) * 96 + 2 * lane]) =
            u32x2{f2bf(a[0] / L) | (f2bf(a[1] / L) << 16), f2bf(a[2] / L) | (f2bf(a[3] / L) << 16)};
    }
    __syncthreads();
    STAMP(6);
    {
      const u32* p0 = &s_o[48 * wave + 6 * r];
      const u32x2 oa0 = *reinterpret_cast<const u32x2*>(p0), oa1 = *reinterpret_cast<const u32x2*>(p0 + 2), oa2 = *reinterpret_cast<const u32x2*>(p0 + 4);
      const u32x2 ob0 = *reinterpret_cast<const u32x2*>(p0 + 384), ob1 = *reinterpret_cast<const u32x2*>(p0 + 386), ob2 = *reinterpret_cast<const u32x2*>(p0 + 388);
      const u32 oa[6] = {oa0[0], oa0[1], oa1[0], oa1[1], oa2[0], oa2[1]};
      const u32 ob[6] = {ob0[0], ob0[1], ob1[0], ob1[1], ob2[0], ob2[1]};
      float y[16];
#pragma unroll
      for (int e = 0; e < 16; ++e) y[e] = 0.f;
#pragma unroll
      for (int i = 0; i < 6; ++i)
#pragma unroll
        for (int k = 0; k < 8; ++k) { y[k] = dot2(wp[i][k], oa[i], y[k]); y[8 + k] = dot2(wp[i][k], ob[i], y[8 + k]); }
      float u[8], v[4];
#pragma unroll
      for (int e = 0; e < 8; ++e) u[e] = red32(y[e], y[e + 8]);
#pragma unroll
      for (int e = 0; e < 4; ++e) v[e] = red16(u[e], u[e + 4]);
      const float t0 = red8(v[0], v[2]), t1 = red8(v[1], v[3]);
      *reinterpret_cast<f32x2*>(&s_y[wave][(lane >> 5) * 64 + 8 * c + 4 * ((lane >> 4) & 1) + 2 * ((lane >> 3) & 1)]) = f32x2{t0, t1};
    }
    __syncthreads();
    if (tid < 32) {
      f32x4 y = {0.f, 0.f, 0.f, 0.f};
#pragma unroll
      for (int w = 0; w < WAVES; ++w) y += *reinterpret_cast<const f32x4*>(&s_y[w][4 * tid]);
      st_g(rws, OFF_H2 + (u32)((g * 2 + (tid >> 4)) * DM + 64 * ct + 4 * (tid & 15)) * 4,
           u32x4{fbits(y[0]) | 1u, fbits(y[1]) | 1u, fbits(y[2]) | 1u, fbits(y[3]) | 1u});
    }
    STAMP(7);
    return;
  }

  const int ci2 = (wave % 3) * 64 + lane;
  u32 xo = OOB;
  if (roleM) { if (wave < 6 && ci2 < 160) xo = (u32)((wave / 3) * DM + 4 * ci2) * 2; }
  else { if (wave == 7 && lane < 8) xo = (u32)(32 * Q + 4 * lane) * 2; }
  const u32 h2off = (xo == OOB) ? OOB : OFF_H2 + xo * 2;
  const u32x2 xres = __builtin_amdgcn_raw_buffer_load_b64(mk_rsrc(x, 2 * DM * 2), xo, 0, 0);
  const u32 bias_raw = roleM ? (u32)b1[32 * ub + (tid & 31)] : (u32)b2[(32 * Q + (tid & 31)) % DM];
  u32 w1p[3][8], w2p[4][8];
#pragma unroll
  for (int k = 0; k < 4; ++k) {
    w1p[0][2 * k] = pack_lo(tw[0][k], tw[1][k]); w1p[0][2 * k + 1] = pack_hi(tw[0][k], tw[1][k]);
    w1p[1][2 * k] = pack_lo(tw[2][k], tw[3][k]); w1p[1][2 * k + 1] = pack_hi(tw[2][k], tw[3][k]);
    w1p[2][2 * k] = tw[4][k] & 0xffffu;          w1p[2][2 * k + 1] = tw[4][k] >> 16;
#pragma unroll
    for (int p2 = 0; p2 < 4; ++p2) {
      w2p[p2][2 * k] = pack_lo(tw[5 + 2 * p2][k], tw[6 + 2 * p2][k]); w2p[p2][2 * k + 1] = pack_hi(tw[5 + 2 * p2][k], tw[6 + 2 * p2][k]);
      PIN(w2p[p2][2 * k]); PIN(w2p[p2][2 * k + 1]);
    }
#pragma unroll
    for (int p1 = 0; p1 < 3; ++p1) { PIN(w1p[p1][2 * k]); PIN(w1p[p1][2 * k + 1]); }
  }
  float hv[4] = {0.f, 0.f, 0.f, 0.f};
  if (roleM ? (wave < 6) : (wave == 7)) {
    u32x4 pa[8];
    for (int spin = 0; spin < SPIN_MAX; ++spin) {
      POLL_FENCE();
#pragma unroll
      for (int q = 0; q < 8; ++q) pa[q] = ld_g(rws, h2off + q * (2 * DM * 4));
      u32 mn4 = min4(pa[0]);
#pragma unroll
      for (int q = 1; q < 8; ++q) mn4 = umin(mn4, min4(pa[q]));
      if (__all((mn4 != 0u) | (h2off == OOB))) break;
      __builtin_amdgcn_s_sleep(1);
    }
#pragma unroll
    for (int q = 0; q < 8; ++q)
#pragma unroll
      for (int k = 0; k < 4; ++k) hv[k] += bitsf(pa[q][k]);
    hv[0] = rbf(bflo(xres[0]) + rbf(hv[0])); hv[1] = rbf(bfhi(xres[0]) + rbf(hv[1]));
    hv[2] = rbf(bflo(xres[1]) + rbf(hv[2])); hv[3] = rbf(bfhi(xres[1]) + rbf(hv[3]));
  }
  STAMP(8);
  if (roleM) {
    const bool act = xo != OOB;
    if (wave < 6) {
      const float sw = wave_sum(act ? (hv[0] + hv[1]) + (hv[2] + hv[3]) : 0.f);
      const float mw = sw * ((wave % 3 == 2) ? (1.0f / 128.f) : (1.0f / 256.f));
      float q2 = 0.f;
#pragma unroll
      for (int k = 0; k < 4; ++k) { const float d = hv[k] - mw; q2 = fmaf(d, d, q2); }
      q2 = wave_sum(act ? q2 : 0.f);
      if (lane == 0) s_st[wave] = f32x2{sw, q2};
    }
    __syncthreads();
    if (wave < 6) {
      const int w0 = (wave / 3) * 3;
      const f32x2 s0 = s_st[w0], s1 = s_st[w0 + 1], s2 = s_st[w0 + 2];
      const float mean = (s0[0] + s1[0] + s2[0]) * (1.0f / DM);
      const float d0 = s0[0] * (1.0f / 256.f) - mean, d1 = s1[0] * (1.0f / 256.f) - mean, d2 = s2[0] * (1.0f / 128.f) - mean;
      const float M2 = (s0[1] + s1[1] + s2[1]) + 256.f * (d0 * d0 + d1 * d1) + 128.f * d2 * d2;
      const float mb = rbf(mean);
      const float den = rbf(sqrtf(rbf(rbf(M2 * (1.0f / DM)) + 1e-5f)));
      const u32 n01 = f2bf(rbf(hv[0] - mb) / den) | (f2bf(rbf(hv[1] - mb) / den) << 16);
      const u32 n23 = f2bf(rbf(hv[2] - mb) / den) | (f2bf(rbf(hv[3] - mb) / den) << 16);
      if (act) *reinterpret_cast<u32x2*>(&s_hn[wave / 3][2 * ci2]) = u32x2{n01, n23};
    }
    __syncthreads();
    {
      const int rr = lane >> 2;
      float a[16];
#pragma unroll
      for (int bb = 0; bb < 2; ++bb) {
        const u32 n0 = s_hn[bb][40 * wave + rr], n1 = s_hn[bb][40 * wave + 16 + rr];
        const u32 n2 = reinterpret_cast<const u16*>(&s_hn[bb][0])[80 * wave + 64 + rr];
#pragma unroll
        for (int k = 0; k < 8; ++k) a[8 * bb + k] = dot2(w1p[2][k], n2, dot2(w1p[1][k], n1, dot2(w1p[0][k], n0, 0.f)));
      }
      float u[8], v[4], w[2];
#pragma unroll
      for (int e = 0; e < 8; ++e) u[e] = red32(a[e], a[e + 8]);
#pragma unroll
      for (int e = 0; e < 4; ++e) v[e] = red16(u[e], u[e + 4]);
#pragma unroll
      for (int e = 0; e < 2; ++e) w[e] = red8(v[e], v[e + 2]);
      const float t = red4(w[0], w[1]);
      s_y[wave][(lane >> 5) * 32 + 8 * (lane & 3) + 4 * ((lane >> 4) & 1) + 2 * ((lane >> 3) & 1) + ((lane >> 2) & 1)] = t;
    }
    __syncthreads();
    if (tid < 64) {
      float sacc = 0.f;
#pragma unroll
      for (int w = 0; w < WAVES; ++w) sacc += s_y[w][tid];
      const float hh = rbf(rbf(sacc) + bf2f(bias_raw));
      const float rr = hh > 0.f ? hh : 0.f;
      s_a[tid >> 5][tid & 31] = (u16)f2bf(rr * rr);
    }
    __syncthreads();
    STAMP(9);
    if (wave < 5) {
      const u32x4 a0 = *reinterpret_cast<const u32x4*>(&s_a[0][8 * (lane >> 4)]);
      const u32x4 a1 = *reinterpret_cast<const u32x4*>(&s_a[1][8 * (lane >> 4)]);
      float y[16];
#pragma unroll
      for (int e = 0; e < 16; ++e) y[e] = 0.f;
#pragma unroll
      for (int p2 = 0; p2 < 4; ++p2)
#pragma unroll
        for (int k = 0; k < 8; ++k) { y[k] = dot2(w2p[p2][k], a0[p2], y[k]); y[8 + k] = dot2(w2p[p2][k], a1[p2], y[8 + k]); }
      float u[8], v[4];
#pragma unroll
      for (int e = 0; e < 8; ++e) u[e] = red32(y[e], y[e + 8]);
#pragma unroll
      for (int e = 0; e < 4; ++e) v[e] = red16(u[e], u[e + 4]);
      st_g(rws, OFF_H3 + (u32)mi * (2 * DM * 4) + (u32)((lane >> 5) * DM + 8 * (16 * wave + (lane & 15)) + 4 * ((lane >> 4) & 1)) * 4,
           u32x4{fbits(v[0]) | 1u, fbits(v[1]) | 1u, fbits(v[2]) | 1u, fbits(v[3]) | 1u});
    }
    STAMP(10);
  } else {
    if (wave == 7) {
      if (lane < 8) *reinterpret_cast<f32x4*>(&s_hres[4 * lane]) = f32x4{hv[0], hv[1], hv[2], hv[3]};
    } else if (wave < 6) {
      u32x4 pw;
      const u32 off = OFF_H3 + (u32)(8 * wave + r) * (2 * DM * 4) + (u32)(32 * Q + 4 * c) * 4;
      for (int spin = 0; spin < SPIN_MAX; ++spin) {
        POLL_FENCE();
        pw = ld_g(rws, off);
        if (__all(min4(pw) != 0u)) break;
        __builtin_amdgcn_s_sleep(1);
      }
      const float u0 = red32(bitsf(pw[0]), bitsf(pw[2])), u1 = red32(bitsf(pw[1]), bitsf(pw[3]));
      float t = red16(u0, u1);
      t += dpp<ROR8>(t);
      if ((lane & 8) == 0) s_y[wave][4 * c + 2 * (lane >> 5) + ((lane >> 4) & 1)] = t;
    }
    STAMP(9);
    __syncthreads();
    if (tid < 32) {
      const float sacc = ((s_y[0][tid] + s_y[1][tid]) + (s_y[2][tid] + s_y[3][tid])) + (s_y[4][tid] + s_y[5][tid]);
      const float o = rbf(rbf(sacc) + bf2f(bias_raw));
      out[32 * Q + tid] = (u16)f2bf(s_hres[tid] + o);
    }
    STAMP(10);
    if (bid == 2 && tid == 0) __builtin_amdgcn_raw_buffer_store_b64(u32x2{tagA, tagB}, rws, OFF_EPOCH, 0, AUX_SC1);
  }
}

extern "C" void kernel_launch(void* const* d_in, const int* in_sizes, int n_in,
                              void* d_out, int out_size, void* d_ws, size_t ws_size,
                              hipStream_t stream) {
  fused_kernel<<<256, 512, 0, stream>>>((const u16*)d_in[0], (const u16*)d_in[1], (const u16*)d_in[2], (const u16*)d_in[3],
                                        (const u16*)d_in[4], (const u16*)d_in[6], (const u16*)d_in[7], (const u16*)d_in[8],
                                        (const u16*)d_in[9], (unsigned char*)d_ws, (u16*)d_out);
}
```

```cpp
#define NCP 1
#define SELO
#define NOEXH
#define STUBS
#define XROLE
#include <hip/hip_runtime.h>
#include <hip/hip_bf16.h>
#include <stdint.h>

typedef unsigned int u32;
typedef unsigned short u16;
typedef unsigned long long u64;
using u32x4 = __attribute__((ext_vector_type(4))) unsigned;
using u32x2 = __attribute__((ext_vector_type(2))) unsigned;
using f32x2 = __attribute__((ext_vector_type(2))) float;
using f32x4 = __attribute__((ext_vector_type(4))) float;
using bf16x2v = __attribute__((ext_vector_type(2))) __bf16;
typedef __amdgpu_buffer_rsrc_t rsrc_t;

constexpr int DM = 640, NH = 32, SK = 8192, DH = 192, FF = 1536;
constexpr int HD = NH * DH;
constexpr int WAVES = 8;
constexpr float SCALE = 0.07216878364870322f;
constexpr float LOG2E = 1.4426950408889634f;

constexpr int RING = 6;
constexpr u32 NU = SK / 16;
#define AR 4
#define LEAD 16
constexpr u32 NSA = 8 * AR, SLAB = SK * DH * 2;
constexpr u32 SENT = 0xffffffffu;
#define NB 3
#define NC 11
#define PD 7
#define PC PD
#define PP 22

constexpr u32 OFF_EPOCH = 0;
constexpr u32 OFF_FLAGS = 1024;
constexpr u32 OFF_CNT = 4096;
constexpr u32 OFF_H0 = 12288;
constexpr u32 OFF_H1 = OFF_H0 + 256 * 3072;
constexpr u32 OFF_H2 = OFF_H1 + 768 * 1024;
constexpr u32 OFF_H3 = OFF_H2 + 16 * 2 * DM * 4;
#define CSTRIDE 4352
static_assert(OFF_H3 + 48 * 2 * DM * 4 <= (2u << 20), "");
constexpr u32 OFF_CNT2 = 2u << 20;
constexpr u32 H2B = 16 * 2 * DM * 4;
constexpr u32 OFF_H2X = 0x250000;
constexpr u32 WS_BYTES = OFF_H2X + 8 * H2B;
static_assert(OFF_CNT2 + 64 * CSTRIDE <= OFF_H2X, "");
constexpr u32 OOB = 0x40000000u;
#define T_M 0u
#define T_O 48u
#define T_F 208u
#define GRP(b) ((u32)((b) & 7))
#define G_M 0u
#define G_O 6u
#define G_F 26u
#define W1AUX AUX_NT
#define SELS 8
constexpr int SPIN_MAX = 1 << 18;
constexpr int AUX_NT = 2, AUX_SC1 = 16;
#define PSLEEP 1
#define POLL1
#define OUT_AUX 16

__device__ __forceinline__ float bf2f(u32 h) { return __builtin_bit_cast(float, h << 16); }
__device__ __forceinline__ float bflo(u32 w) { return __builtin_bit_cast(float, w << 16); }
__device__ __forceinline__ float bfhi(u32 w) { return __builtin_bit_cast(float, w & 0xffff0000u); }
__device__ __forceinline__ float rbf(float x) { return __bfloat162float(__float2bfloat16(x)); }
__device__ __forceinline__ u32 f2bf(float x) { return (u32)__builtin_bit_cast(u16, __float2bfloat16(x)); }
__device__ __forceinline__ u32 fbits(float x) { return __builtin_bit_cast(u32, x); }
__device__ __forceinline__ float bitsf(u32 x) { return __builtin_bit_cast(float, x); }

__device__ __forceinline__ float dot2(u32 k, u32 q, float acc) {
  return __builtin_amdgcn_fdot2_f32_bf16(__builtin_bit_cast(bf16x2v, k), __builtin_bit_cast(bf16x2v, q), acc, false);
}
__device__ __forceinline__ u32 pack_lo(u32 a, u32 b) { return __builtin_amdgcn_perm(b, a, 0x05040100u); }
__device__ __forceinline__ u32 pack_hi(u32 a, u32 b) { return __builtin_amdgcn_perm(b, a, 0x07060302u); }

template <int CTRL> __device__ __forceinline__ float dpp(float x) {
  return __builtin_bit_cast(float, __builtin_amdgcn_mov_dpp(__builtin_bit_cast(int, x), CTRL, 0xf, 0xf, true));
}
template <int CTRL, int BANK> __device__ __forceinline__ float dpp_upd(float old, float x) {
  return __builtin_bit_cast(float, __builtin_amdgcn_update_dpp(__builtin_bit_cast(int, old), __builtin_bit_cast(int, x), CTRL, 0xf, BANK, false));
}
template <int CTRL> __device__ __forceinline__ float max_dpp(float x) {
  return __builtin_elementwise_maximum(x, dpp<CTRL>(x));
}
constexpr int XOR1 = 0xB1, XOR2 = 0x4E, HMIR = 0x141, ROR8 = 0x128, ROR4 = 0x124, ROR12 = 0x12C, IDENT = 0xE4;

__device__ __forceinline__ float wave_max(float x) {
  x = fmaxf(x, dpp<XOR1>(x));
  x = fmaxf(x, dpp<XOR2>(x));
  x = fmaxf(x, dpp<HMIR>(x));
  x = fmaxf(x, dpp<ROR8>(x));
  auto s = __builtin_amdgcn_permlane16_swap(__float_as_uint(x), __float_as_uint(x), false, false);
  x = fmaxf(__uint_as_float(s[0]), __uint_as_float(s[1]));
  auto t = __builtin_amdgcn_permlane32_swap(__float_as_uint(x), __float_as_uint(x), false, false);
  return fmaxf(__uint_as_float(t[0]), __uint_as_float(t[1]));
}
__device__ __forceinline__ float wave_sum(float x) {
  x += dpp<XOR1>(x);
  x += dpp<XOR2>(x);
  x += dpp<HMIR>(x);
  x += dpp<ROR8>(x);
  auto s = __builtin_amdgcn_permlane16_swap(__float_as_uint(x), __float_as_uint(x), false, false);
  x = __uint_as_float(s[0]) + __uint_as_float(s[1]);
  auto t = __builtin_amdgcn_permlane32_swap(__float_as_uint(x), __float_as_uint(x), false, false);
  return __uint_as_float(t[0]) + __uint_as_float(t[1]);
}
__device__ __forceinline__ float red32(float a, float b) {
  auto s = __builtin_amdgcn_permlane32_swap(__float_as_uint(a), __float_as_uint(b), false, false);
  return __uint_as_float(s[0]) + __uint_as_float(s[1]);
}
__device__ __forceinline__ float red16(float a, float b) {
  auto s = __builtin_amdgcn_permlane16_swap(__float_as_uint(a), __float_as_uint(b), false, false);
  return __uint_as_float(s[0]) + __uint_as_float(s[1]);
}
__device__ __forceinline__ float red8(float a, float b) { return dpp_upd<ROR8, 0xC>(a, b) + dpp_upd<ROR8, 0x3>(b, a); }
__device__ __forceinline__ float red4m(float a, float b) { return dpp_upd<HMIR, 0xA>(a, b) + dpp_upd<HMIR, 0x5>(b, a); }
__device__ __forceinline__ float red4(float a, float b) {
  return dpp_upd<IDENT, 0xA>(a, b) + dpp_upd<ROR4, 0xA>(dpp_upd<ROR12, 0x5>(a, a), b);
}
__device__ __forceinline__ float red2(float a, float b, int lane) { const float t = a + dpp<XOR2>(a), u = b + dpp<XOR2>(b); return (lane & 2) ? u : t; }
__device__ __forceinline__ float red1(float a, float b, int lane) { const float t = a + dpp<XOR1>(a), u = b + dpp<XOR1>(b); return (lane & 1) ? u : t; }

__device__ __forceinline__ void tr_reduce24(float (&a)[24], float (&o)[3]) {
  float b[12], c[6];
#pragma unroll
  for (int v = 0; v < 12; ++v) b[v] = red32(a[v], a[v + 12]);
#pragma unroll
  for (int v = 0; v < 6; ++v) c[v] = red16(b[v], b[v + 6]);
#pragma unroll
  for (int v = 0; v < 3; ++v) o[v] = red8(c[v], c[v + 3]);
}
using bf16x8v = __attribute__((ext_vector_type(8))) __bf16;
using s16x4 = __attribute__((ext_vector_type(4))) short;
__device__ __forceinline__ f32x4 mfma32(u32x4 a, u32x4 b, f32x4 c) {
  return __builtin_amdgcn_mfma_f32_16x16x32_bf16(__builtin_bit_cast(bf16x8v, a), __builtin_bit_cast(bf16x8v, b), c, 0, 0, 0);
}
__device__ __forceinline__ f32x4 mfma16(u32x2 a, u32x2 b, f32x4 c) {
  return __builtin_amdgcn_mfma_f32_16x16x16bf16_1k(__builtin_bit_cast(s16x4, a), __builtin_bit_cast(s16x4, b), c, 0, 0, 0);
}
__device__ __forceinline__ rsrc_t mk_rsrc(const void* p, u32 bytes) { return __builtin_amdgcn_make_buffer_rsrc((void*)p, 0, bytes, 0x00020000); }

template <int N> __device__ __forceinline__ u32x4 issue(const rsrc_t r, const u32 (&vo)[2]) {
  return __builtin_amdgcn_raw_buffer_load_b128(r, vo[N / 3], (N % 3) * 128, AUX_NT);
}
template <int I> struct IC { static constexpr int value = I; };
template <int A, int B, bool PACED, class F> __device__ __forceinline__ void run_seq(F& f) {
  if constexpr (A < B) {
    __builtin_amdgcn_sched_barrier(0);
    if constexpr (PACED) __builtin_amdgcn_s_waitcnt(((PD - 1) & 15) | (7 << 4) | (15 << 8) | (((PD - 1) >> 4) << 14));
    __builtin_amdgcn_sched_barrier(0);
    f(IC<A>{});
    __builtin_amdgcn_sched_barrier(0);
    run_seq<A + 1, B, PACED>(f);
  }
}
__device__ __forceinline__ void fma8(float* acc, const u32x4 w, const float p) {
#pragma unroll
  for (int k = 0; k < 4; ++k) {
    acc[2 * k] = fmaf(p, bflo(w[k]), acc[2 * k]);
    acc[2 * k + 1] = fmaf(p, bfhi(w[k]), acc[2 * k + 1]);
  }
  asm volatile("" : "+v"(acc[0]), "+v"(acc[1]), "+v"(acc[2]), "+v"(acc[3]), "+v"(acc[4]), "+v"(acc[5]), "+v"(acc[6]), "+v"(acc[7]));
}
template <int N> __device__ __forceinline__ float kdot(u32x4 (&ring)[RING], const rsrc_t rn, const u32 (&vo)[2], const u32x4 q, float d) {
#pragma unroll
  for (int k = 0; k < 4; ++k) d = dot2(ring[N][k], q[k], d);
  asm volatile("" : "+v"(d));
  ring[N] = issue<N>(rn, vo);
  __builtin_amdgcn_sched_barrier(0);
  return d;
}
template <int N> __device__ __forceinline__ void vfma(u32x4 (&ring)[RING], const rsrc_t rn, const u32 (&vo)[2], float* acc, float p) {
  fma8(acc, ring[N], p);
  ring[N] = issue<N>(rn, vo);
  __builtin_amdgcn_sched_barrier(0);
}

__device__ __forceinline__ u32x4 ld_g(rsrc_t r, u32 off) { return __builtin_amdgcn_raw_buffer_load_b128(r, off, 0, AUX_SC1); }
__device__ __forceinline__ void st_g(rsrc_t r, u32 off, u32x4 v) { __builtin_amdgcn_raw_buffer_store_b128(v, r, off, 0, AUX_SC1); }
__device__ __forceinline__ u32 sel(bool p, u32 a, u32 b) { return p ? a : b; }
__device__ __forceinline__ u32 umin(u32 a, u32 b) { return a < b ? a : b; }
__device__ __forceinline__ u32 min4(const u32x4 v) { return umin(umin(v[0], v[1]), umin(v[2], v[3])); }
#define POLL_FENCE() asm volatile("" ::: "memory")
#define PIN(x) asm volatile("" : "+v"(x))
template <int N> __device__ __forceinline__ void poll_issue(u32x4 (&p)[N], rsrc_t r, u32 off, u32 stride) {
  POLL_FENCE();
#pragma unroll
  for (int q = 0; q < N; ++q) p[q] = ld_g(r, off + q * stride);
  __builtin_amdgcn_sched_barrier(0);
}
template <int N> __device__ __forceinline__ bool poll_ok(const u32x4 (&p)[N], bool idle) {
  u32 mn = min4(p[0]);
#pragma unroll
  for (int q = 1; q < N; ++q) mn = umin(mn, min4(p[q]));
  return __all((mn != 0u) | idle);
}
template <int N> __device__ __forceinline__ void poll(u32x4 (&a)[N], rsrc_t r, u32 off, u32 stride) {
  const bool idle = off >= OOB;
  for (int spin = 0; spin < SPIN_MAX; ++spin) {
    poll_issue<N>(a, r, off, stride);
    if (poll_ok<N>(a, idle)) break;
    __builtin_amdgcn_s_sleep(PSLEEP);
  }
}
#define VISAT 1
#define VISW 7
#define LOOPK 1024
#define STR2(x) #x
#define STR(x) STR2(x)
#define PAGE0(name) asm volatile("s_branch " name "_go\n" name "_stub:\ns_setpc_b64 s[98:99]\n" name "_go:\n" ::: "memory")
#define PAGE(name) asm volatile("s_branch " name "_go\n.p2align 12\n" name "_stub:\ns_setpc_b64 s[98:99]\n" name "_go:\n" ::: "memory")
#define PAGE_AT(k) do { } while (0)
#define VISIT(name) asm volatile("s_call_b64 s[98:99], " name "_stub\n" ::: "s98", "s99", "memory")
#define STAMP(i) do { } while (0)
#define DUMP() do { } while (0)

#define LSTAMP(i) STAMP(i)
struct __attribute__((aligned(16))) Smem {
  float s_big[20 * 2 * DH];
  float s_y[WAVES][128];
  u32 s_xn[2][20][2];
  u32 s_hn[2][DM / 2];
  u16 s_a[WAVES][2][32];
  float s_S[WAVES][32];
  float s_bias[32];
  u16 s_q[2][DH];
  u32 s_sc[128][16];
  u32x4 s_wb[4][6][64];
  float s_hres[32];
  float s_m[WAVES], s_l[WAVES];
  f32x2 s_st[2 * WAVES];
  u32 s_cnt[64];
  u32 s_qn, s_w, s_nv, s_ex, s_role, s_dn, s_exb, s_pad;
  f32x2 s_ml[8], s_mlb[8];
  u32 s_stamp[2];
  u32 s_tab[64];
};
__global__ void __launch_bounds__(512, 2)
fused_kernel(const u16* __restrict__ x, const u16* __restrict__ kc, const u16* __restrict__ vc,
             const u16* __restrict__ wq, const u16* __restrict__ wo, const u16* __restrict__ w1, const u16* __restrict__ b1,
             const u16* __restrict__ w2, const u16* __restrict__ b2, unsigned char* ws, u16* __restrict__ out) {
  __shared__ Smem sm;
  auto& s_big = sm.s_big; auto& s_y = sm.s_y; auto& s_xn = sm.s_xn; auto& s_hn = sm.s_hn; auto& s_a = sm.s_a; auto& s_q = sm.s_q;
  auto& s_hres = sm.s_hres; auto& s_m = sm.s_m; auto& s_l = sm.s_l; auto& s_st = sm.s_st; auto& s_cnt = sm.s_cnt; auto& s_S = sm.s_S; auto& s_bias = sm.s_bias;

  const int bid = blockIdx.x;
#define XMAP 4
  const int vid = (bid & ~6) | ((bid & 2) << 1) | ((bid >> 1) & 2);
  const int h = vid >> 3, e8 = vid & 7;
  const int Q = vid >> 2, sq4 = vid & 3;
  const int tid = threadIdx.x, lane = tid & 63;
  const int wave = __builtin_amdgcn_readfirstlane(tid >> 6);
  const int r = lane >> 3, c = lane & 7;

  STAMP(0);
  const rsrc_t rws = mk_rsrc(ws, WS_BYTES);
  const u32x4 rsw = {(u32)(size_t)ws, (u32)((size_t)ws >> 32), WS_BYTES, 0x00020000u};
#define PH 317
  const bool sp = bid >= 252;
  const u32 ta = (u32)bid / 12u, slabB = (u32)bid / 6u;
  const u32 my_h1 = OFF_H1 + (bid + (sp ? 256u : 516u)) * 1024u, my_h1b = OFF_H1 + (bid + 6u * slabB) * 1024u;
  {
    u32 zo = OOB;
    if (wave == 7) { if (lane < 49) zo = my_h1 + lane * 16; else if (lane == 63 && e8 == 0) zo = OFF_CNT2 + (u32)h * CSTRIDE; else if (lane >= 52 && lane < 60 && bid == 0) zo = OFF_CNT + (lane - 52) * 1024; }
    else if (wave == 6) { if (lane < 49) zo = my_h1b + lane * 16; }
    else if (wave < 5) {
      if (sq4 == 1 && Q < 48) zo = OFF_H3 + (u32)Q * (2 * DM * 4) + tid * 16;
      else if ((sq4 & 1) == 0) zo = OFF_H2X + (u32)((2 * Q + (sq4 >> 1)) * 320 + tid) * 16;
    }
    st_g(rws, zo, u32x4{0u, 0u, 0u, 0u});
  }
  const u32x2 epoch = __builtin_amdgcn_raw_buffer_load_b64(rws, OFF_EPOCH, 0, AUX_SC1);
  const u16* xb = x + (wave & 1) * DM;
  u32 xs[5];
#pragma unroll
  for (int i = 0; i < 5; ++i) xs[i] = reinterpret_cast<const u32*>(xb)[lane + 64 * i];
  const int xrow = (wave >> 1) * 20 + (lane < 20 ? lane : 0);
  u32 xr = reinterpret_cast<const u32*>(xb)[(80 * e8 + xrow) >> 1];
  const int ci = tid % 24, rg = tid / 24;
  u32x4 wqv[4];
  {
    const rsrc_t rw = mk_rsrc(wq + (size_t)(80 * e8) * HD + h * DH, (79 * HD + DH) * 2);
    const u32 vo = (tid < 480) ? (u32)(rg * HD * 2 + ci * 16) : OOB;
#pragma unroll
    for (int i = 0; i < 4; ++i) wqv[i] = __builtin_amdgcn_raw_buffer_load_b128(rw, vo, i * 20 * HD * 2, AUX_NT);
  }
  __builtin_amdgcn_sched_barrier(0);

  const char* kb = reinterpret_cast<const char*>(kc);
  const char* vb = reinterpret_cast<const char*>(vc);
  const u32 vo[2] = {(u32)(r * 384 + c * 16), (u32)(3072 + r * 384 + c * 16)};
  const u32 slabA = sp ? 42u : 43u + ta;
  const u32 jA = bid - 12u * ta, jB = bid - 6u * slabB;
  const u32 sA = sp ? 32u : 96u, TB = sp ? 4u : 6u;
  const u32 nB = (sp | (jB < 2u)) ? 86u : 85u;
  const u32 nch = sp ? 21u - 4u * AR : 64u - 12u * AR;
  const u32 cnt_off = OFF_CNT2 + (slabA - 32u) * CSTRIDE;
  const u32 cP = sp ? 344u : slabA * PH, cA = cP + 8u * jA, cT = (sp ? 0u : slabB * PH) + jB, cB = cT + 7u * jB;
  const u32 e1 = NSA + nB, e2 = e1 + nB, nloc = e2 + NSA;
  auto offK = [&](int k) { return k < AR ? slabA * SLAB + ((cA + sA * k + wave) & (NU - 1u)) * 6144u : slabB * SLAB + ((cB + 8u * TB * (k - AR) + wave) & (NU - 1u)) * 6144u; };
#define XU 0
  u32x4 tw[12], pf[12], xu[6 * XU + 1], ring[RING];
  const rsrc_t rk0 = mk_rsrc(kb + offK(0), 6144), rk1 = mk_rsrc(kb + offK(1), 6144), rk2 = mk_rsrc(kb + offK(2), 6144);
  const rsrc_t rk3 = mk_rsrc(kb + offK(3), 6144), rk4 = mk_rsrc(kb + offK(4), 6144);
  const rsrc_t rk5 = mk_rsrc(kb + offK(5), 6144), rk6 = mk_rsrc(kb + offK(6), 6144);
  auto seq = [&](auto ic) {
    constexpr int I = decltype(ic)::value;
    if constexpr (I < 6) ring[I] = issue<I>(rk0, vo);
    else if constexpr (I < 12) pf[I - 6] = issue<I - 6>(rk1, vo);
    else if constexpr (I < 18) pf[I - 6] = issue<I - 12>(rk2, vo);
    else if constexpr (I < 24) tw[I - 18] = issue<I - 18>(rk3, vo);
    else if constexpr (I < 30) tw[I - 18] = issue<I - 24>(rk4, vo);
    else if constexpr (I < 36) xu[I - 30] = issue<I - 30>(rk5, vo);
    else if constexpr (I < 42) xu[I - 30] = issue<I - 36>(rk6, vo);
  };
  if (tid == 0) { s_cnt[0] = 48u + 8u * XU; sm.s_qn = 0u; sm.s_w = 0u; sm.s_nv = 0u; sm.s_ex = 0u; sm.s_dn = 0u; sm.s_exb = 0u; sm.s_role = 0u; }
  if (wave == 1) sm.s_tab[lane] = 0u;
  reinterpret_cast<u32x4*>(&sm.s_sc[0][0])[tid] = u32x4{SENT, SENT, SENT, SENT};
  __builtin_amdgcn_sched_barrier(0);
  __builtin_amdgcn_s_barrier();
  run_seq<0, NB, false>(seq);
  PIN(xr);
  xr = (xrow & 1) ? (xr >> 16) : (xr & 0xffffu);

  {
    float sum = 0.f;
#pragma unroll
    for (int i = 0; i < 5; ++i) sum += bflo(xs[i]) + bfhi(xs[i]);
    sum = wave_sum(sum);
    const float mean = sum * (1.0f / DM);
    float sq = 0.f;
#pragma unroll
    for (int i = 0; i < 5; ++i) { float a = bflo(xs[i]) - mean, bb = bfhi(xs[i]) - mean; sq += a * a + bb * bb; }
    sq = wave_sum(sq);
    const float mb = rbf(mean), vb = rbf(sq * (1.0f / DM));
    const float den = rbf(sqrtf(rbf(vb + 1e-5f)));
    const u32 xn = f2bf(rbf(bf2f(xr) - mb) / den);
    if (lane < 20) reinterpret_cast<u16*>(&s_xn[wave & 1][lane][0])[wave >> 1] = (u16)xn;
  }
  __syncthreads();
  STAMP(1);
  const u32 tagA = epoch[0] + 1u, tagB = epoch[1] + 1u;
  { u32 w3 = wqv[3][0]; PIN(w3); wqv[3][0] = w3; }
  run_seq<NB, NC, false>(seq);
  {
    const int rgc = rg < 20 ? rg : 0;
    const u32x2 xa = *reinterpret_cast<const u32x2*>(&s_xn[0][rgc][0]);
    const u32x2 xc = *reinterpret_cast<const u32x2*>(&s_xn[1][rgc][0]);
    float a0[8], a1[8];
#pragma unroll
    for (int k = 0; k < 4; ++k) {
      const u32 p01l = pack_lo(wqv[0][k], wqv[1][k]), p01h = pack_hi(wqv[0][k], wqv[1][k]);
      const u32 p23l = pack_lo(wqv[2][k], wqv[3][k]), p23h = pack_hi(wqv[2][k], wqv[3][k]);
      a0[2 * k] = dot2(p23l, xa[1], dot2(p01l, xa[0], 0.f));
      a0[2 * k + 1] = dot2(p23h, xa[1], dot2(p01h, xa[0], 0.f));
      a1[2 * k] = dot2(p23l, xc[1], dot2(p01l, xc[0], 0.f));
      a1[2 * k + 1] = dot2(p23h, xc[1], dot2(p01h, xc[0], 0.f));
    }
    if (tid < 480) {
      float* d0 = &s_big[(rg * 2 + 0) * DH + ci * 8];
      float* d1 = &s_big[(rg * 2 + 1) * DH + ci * 8];
      *reinterpret_cast<f32x4*>(d0) = f32x4{a0[0], a0[1], a0[2], a0[3]};
      *reinterpret_cast<f32x4*>(d0 + 4) = f32x4{a0[4], a0[5], a0[6], a0[7]};
      *reinterpret_cast<f32x4*>(d1) = f32x4{a1[0], a1[1], a1[2], a1[3]};
      *reinterpret_cast<f32x4*>(d1 + 4) = f32x4{a1[4], a1[5], a1[6], a1[7]};
    }
  }
  __syncthreads();
  if (tid < 192) {
    float v0 = 0.f, v1 = 0.f;
#pragma unroll
    for (int k = 0; k < 20; ++k) { const f32x2 a = *reinterpret_cast<const f32x2*>(&s_big[k * 2 * DH + 2 * tid]); v0 += a[0]; v1 += a[1]; }
    st_g(rws, OFF_H0 + (u32)vid * 3072 + tid * 16, u32x4{fbits(v0), tagA, fbits(v1), tagB});
  } else if (tid == 448) {
    __builtin_amdgcn_raw_buffer_store_b64(u32x2{tagA, tagB}, rws, OFF_FLAGS + bid * 8, 0, AUX_SC1);
  }
  STAMP(2);
  run_seq<NC, PP, true>(seq);
  const u32 qs = sel(wave < 4, slabA, slabB);
  const u32 qoff = OFF_H0 + ((qs & 31u) * 8u + r) * 3072u + ((qs >> 5) * 96u + 24u * (wave & 3) + c) * 16u;
  u32x4 pq[3];
  auto fetch = [&]() {
    POLL_FENCE();
#pragma unroll
    for (int k = 0; k < 3; ++k) pq[k] = ld_g(rws, qoff + k * 128);
  };
  auto good = [&]() {
    u32 bad = 0u;
#pragma unroll
    for (int k = 0; k < 3; ++k) bad |= (pq[k][1] ^ tagA) | (pq[k][3] ^ tagB);
    return __all(bad == 0u);
  };
  auto publish = [&](u32 inc) {
    if (!good()) {
      for (int spin = 0; spin < SPIN_MAX; ++spin) { fetch(); if (good()) break; }
    }
#pragma unroll
    for (int k = 0; k < 3; ++k) {
      float u = red32(bitsf(pq[k][0]), bitsf(pq[k][2]));
      u = red16(u, u);
      u += dpp<ROR8>(u);
      if ((lane & 24) == 0) s_q[0][2 * (24 * wave + 8 * k + c) + (lane >> 5)] = (u16)f2bf(u);
    }
    POLL_FENCE();
    if (lane == 0) __hip_atomic_fetch_add(&sm.s_qn, inc, __ATOMIC_RELAXED, __HIP_MEMORY_SCOPE_WORKGROUP);
  };
  auto qwait = [&](u32 bit) {
#pragma unroll 1
    for (int spin = 0; spin < SPIN_MAX * 4; ++spin) {
      POLL_FENCE();
      if (__builtin_amdgcn_readfirstlane(__hip_atomic_load(&sm.s_qn, __ATOMIC_RELAXED, __HIP_MEMORY_SCOPE_WORKGROUP)) & bit) break;
      __builtin_amdgcn_s_sleep(1);
    }
    POLL_FENCE();
  };
  fetch();
  run_seq<PP, PP + PC, true>(seq);
  if (wave < 4) publish(1u);
  run_seq<PP + PC, 30 + 6 * XU, true>(seq);
  if (wave == VISW) {
    VISIT("pg_o"); VISIT("pg_m"); VISIT("pg_f");
  }
  qwait(4u);
  u32x4 qf[3], qa[3];
#pragma unroll
  for (int j = 0; j < 3; ++j) qa[j] = *reinterpret_cast<const u32x4*>(&s_q[0][j * 64 + c * 8]);
  __builtin_amdgcn_sched_barrier(0);

  STAMP(3);
  float m = -INFINITY, l = 0.f;
  float acc[24];
#pragma unroll
  for (int n = 0; n < 24; ++n) acc[n] = 0.f;
  const u32 vo_cnt = (lane == 0) ? cnt_off : OOB;
  char* const smb = reinterpret_cast<char*>(&sm);
  u32 a_sc = (u32)__builtin_offsetof(Smem, s_sc) + (u32)((c >> 2) * 8 + r) * 4u;
  PIN(a_sc);
  auto put = [&](float d0, float d1, u32 slot) {
    float sc = red4m(d0, d1);
    sc += dpp<XOR2>(sc);
    sc += dpp<XOR1>(sc);
    sc = rbf(rbf(sc) * SCALE);
    __hip_atomic_store(reinterpret_cast<u32*>(smb + a_sc + 64u * slot), fbits(sc), __ATOMIC_RELAXED, __HIP_MEMORY_SCOPE_WORKGROUP);
    POLL_FENCE();
  };
  auto kscore = [&](const u32x4* w, const u32x4* q, u32 slot) {
    float e0 = 0.f, e1_ = 0.f;
#pragma unroll
    for (int jj = 0; jj < 3; ++jj)
#pragma unroll
      for (int k = 0; k < 4; ++k) { e0 = dot2(w[jj][k], q[jj][k], e0); e1_ = dot2(w[3 + jj][k], q[jj][k], e1_); }
    put(e0, e1_, slot);
  };
  u32* const tkp = &s_cnt[lane];
  u32 tk = 0u, li, lk;
  bool req;
  u32* const wp_ = lane == 0 ? &sm.s_w : &s_cnt[lane];
  u32* const np_ = lane == 0 ? &sm.s_nv : &s_cnt[lane];
  auto draw = [&](bool more) {
    lk = __hip_atomic_fetch_add(tkp, more ? 1u : 0u, __ATOMIC_RELAXED, __HIP_MEMORY_SCOPE_WORKGROUP);
  };
  constexpr u32 FK = 256u, FA = 512u, FP = 1024u;
  u32 nty = (40u + 8u * XU + wave) | FK, noff = offK(5 + XU), dty, doff;
  auto ask = [&](bool more) {
    li = __builtin_amdgcn_readfirstlane(lk);
    req = more & ((li == nloc - LEAD) | ((li >= nloc) & (((li - nloc) & 7u) == 0u)));
    if (req) {
      const u32 old = __builtin_amdgcn_readfirstlane(__hip_atomic_fetch_add(wp_, 1u, __ATOMIC_RELAXED, __HIP_MEMORY_SCOPE_WORKGROUP));
      if (old >> 16) { __hip_atomic_fetch_add(wp_, ~0u, __ATOMIC_RELAXED, __HIP_MEMORY_SCOPE_WORKGROUP); req = false; }
    }
    asm volatile("" :: "v"(tk));
    tk = __builtin_amdgcn_raw_ptr_buffer_atomic_add_i32(1, rws, req ? vo_cnt : OOB, 0, AUX_SC1);
    __builtin_amdgcn_sched_barrier(0);
    const bool nA = li >= e2, nK = li < e1;
    const u32 i = li - sel(nK, NSA, sel(nA, e2, e1));
    const bool rest = !nA & (i >= 80u);
    u32 b0 = sel(nA, cA, sel(rest, cT, cB)), mu = sel(nA, sA, sel(rest, TB, 8u * TB)), xx = sel(rest, i, i >> 3), yy = sel(rest, 0u, i & 7u), ba = sel(nA, slabA, slabB);
    asm volatile("" : "+s"(b0), "+s"(mu), "+s"(xx), "+s"(yy), "+s"(ba));
    dty = sel(more, i + sel(nA, FA, sel(nK, NSA + FK, NSA)), nty & ~(FK | FP));
    doff = ba * SLAB + ((b0 + mu * xx + yy) & (NU - 1u)) * 6144u;
    asm volatile("" : "+s"(doff));
    doff = sel(more, doff, noff);
    li = sel(more, li, 0u);
    asm volatile("" : "+s"(dty), "+s"(doff), "+s"(li));
  };
  auto resolve = [&]() {
    if (__builtin_expect(req | (li >= nloc), 0)) {
      if (req) {
        PIN(tk);
        const u32 t = __builtin_amdgcn_readfirstlane(tk);
        if (t < nch) {
          const u32 pos = __builtin_amdgcn_readfirstlane(__hip_atomic_fetch_add(np_, 1u, __ATOMIC_RELAXED, __HIP_MEMORY_SCOPE_WORKGROUP));
          __hip_atomic_store(&sm.s_tab[pos & 63u], t + 1u, __ATOMIC_RELAXED, __HIP_MEMORY_SCOPE_WORKGROUP);
        }
        POLL_FENCE();
        if (t + 1u >= nch) __hip_atomic_fetch_or(wp_, 1u << 16, __ATOMIC_RELAXED, __HIP_MEMORY_SCOPE_WORKGROUP);
        POLL_FENCE();
        __hip_atomic_fetch_add(wp_, ~0u, __ATOMIC_RELAXED, __HIP_MEMORY_SCOPE_WORKGROUP);
      }
      POLL_FENCE();
      if (li >= nloc) {
        u32 v = 0u;
#pragma unroll 1
        for (int spin = 0; spin < SPIN_MAX * 4; ++spin) {
          POLL_FENCE();
          const u32 w = __hip_atomic_load(&sm.s_w, __ATOMIC_RELAXED, __HIP_MEMORY_SCOPE_WORKGROUP);
          POLL_FENCE();
          v = __builtin_amdgcn_readfirstlane(__hip_atomic_load(&sm.s_tab[((li - nloc) >> 3) & 63u], __ATOMIC_RELAXED, __HIP_MEMORY_SCOPE_WORKGROUP));
          const u32 ws_ = __builtin_amdgcn_readfirstlane(w);
          if ((v != 0u) | (ws_ == (1u << 16))) break;
          __builtin_amdgcn_s_sleep(1);
        }
        dty = (120u + wave) | FK | FA | FP;
        doff = v == 0u ? OOB : slabA * SLAB + ((cP + sA * AR + 8u * (v - 1u) + ((li - nloc) & 7u)) & (NU - 1u)) * 6144u;
      }
    }
    nty = dty; noff = doff;
  };
  auto next_rsrc = [&]() { return mk_rsrc(((nty & FK) ? kb : vb) + (noff & (OOB - 1u)), noff < OOB ? 6144u : 0u); };
  auto kunit = [&](const rsrc_t rn, const u32x4* q, u32 slot, bool more) {
    draw(more);
    __builtin_amdgcn_sched_barrier(0);
    float d0 = kdot<0>(ring, rn, vo, q[0], 0.f);
    ask(more);
    __builtin_amdgcn_sched_barrier(0);
    d0 = kdot<1>(ring, rn, vo, q[1], d0);
    d0 = kdot<2>(ring, rn, vo, q[2], d0);
    float d1 = kdot<3>(ring, rn, vo, q[0], 0.f);
    d1 = kdot<4>(ring, rn, vo, q[1], d1);
    d1 = kdot<5>(ring, rn, vo, q[2], d1);
    put(d0, d1, slot);
  };
  const u32 vo_flag = OFF_FLAGS + lane * 16;
  u32x4 flag[2];
  u32 fok = 0u;
  auto flags_ok = [&]() -> bool {
    return __all((((flag[0][0] ^ tagA) | (flag[0][1] ^ tagB) | (flag[0][2] ^ tagA) | (flag[0][3] ^ tagB)) |
                  ((flag[1][0] ^ tagA) | (flag[1][1] ^ tagB) | (flag[1][2] ^ tagA) | (flag[1][3] ^ tagB))) == 0u);
  };
  auto flags_wait = [&]() {
    if (__builtin_expect(fok == 0u, 0)) {
#pragma unroll 1
      for (int spin = 0; spin < SPIN_MAX; ++spin) {
        POLL_FENCE();
        flag[0] = ld_g(rws, vo_flag); flag[1] = ld_g(rws, vo_flag + 1024);
        if (flags_ok()) break;
        __builtin_amdgcn_s_sleep(4);
      }
    }
  };
  u32 a_ex = lane == 0 ? (u32)__builtin_offsetof(Smem, s_ex) : (u32)__builtin_offsetof(Smem, s_cnt) + 4u * lane;
  u32 a_ml = (u32)__builtin_offsetof(Smem, s_ml) + 8u * (lane & 7), a_part = (u32)__builtin_offsetof(Smem, s_big) + 16u * lane, one = 1u;
  PIN(a_ex); PIN(a_ml); PIN(a_part); PIN(one);
  u32 h1o = (lane & 24) == 24 ? (lane == 24 ? my_h1 + 768u : OOB) : my_h1 + (u32)(((lane & 16) ? 32 : 2 * (lane & 8)) + 2 * c + (lane >> 5)) * 16;
  PIN(h1o);
#define RING_USE() asm volatile("" :: "v"(ring[0]), "v"(ring[1]), "v"(ring[2]), "v"(ring[3]), "v"(ring[4]), "v"(ring[5]))
  float t8[8];
  f32x4 o4;
  auto red_a = [&]() {
  asm volatile("s_nop 1\n"
               "v_add_f32_dpp %0, %8, %8 row_ror:8 row_mask:0xf bank_mask:0x3\n v_add_f32_dpp %1, %9, %9 row_ror:8 row_mask:0xf bank_mask:0x3\n"
               "v_add_f32_dpp %2, %10, %10 row_ror:8 row_mask:0xf bank_mask:0x3\n v_add_f32_dpp %3, %11, %11 row_ror:8 row_mask:0xf bank_mask:0x3\n"
               "v_add_f32_dpp %4, %12, %12 row_ror:8 row_mask:0xf bank_mask:0x3\n v_add_f32_dpp %5, %13, %13 row_ror:8 row_mask:0xf bank_mask:0x3\n"
               "v_add_f32_dpp %6, %14, %14 row_ror:8 row_mask:0xf bank_mask:0x3\n v_add_f32_dpp %7, %15, %15 row_ror:8 row_mask:0xf bank_mask:0x3\n"
               "v_add_f32_dpp %0, %16, %16 row_ror:8 row_mask:0xf bank_mask:0xc\n v_add_f32_dpp %1, %17, %17 row_ror:8 row_mask:0xf bank_mask:0xc\n"
               "v_add_f32_dpp %2, %18, %18 row_ror:8 row_mask:0xf bank_mask:0xc\n v_add_f32_dpp %3, %19, %19 row_ror:8 row_mask:0xf bank_mask:0xc\n"
               "v_add_f32_dpp %4, %20, %20 row_ror:8 row_mask:0xf bank_mask:0xc\n v_add_f32_dpp %5, %21, %21 row_ror:8 row_mask:0xf bank_mask:0xc\n"
               "v_add_f32_dpp %6, %22, %22 row_ror:8 row_mask:0xf bank_mask:0xc\n v_add_f32_dpp %7, %23, %23 row_ror:8 row_mask:0xf bank_mask:0xc\n"
               : "=&v"(t8[0]), "=&v"(t8[1]), "=&v"(t8[2]), "=&v"(t8[3]), "=&v"(t8[4]), "=&v"(t8[5]), "=&v"(t8[6]), "=&v"(t8[7])
               : "v"(acc[0]), "v"(acc[1]), "v"(acc[2]), "v"(acc[3]), "v"(acc[4]), "v"(acc[5]), "v"(acc[6]), "v"(acc[7]),
                 "v"(acc[8]), "v"(acc[9]), "v"(acc[10]), "v"(acc[11]), "v"(acc[12]), "v"(acc[13]), "v"(acc[14]), "v"(acc[15]));
  asm volatile("v_add_f32_dpp %8, %8, %8 row_ror:8 row_mask:0xf bank_mask:0xf\n"
               "v_add_f32_dpp %0, %0, %0 row_ror:8 row_mask:0xf bank_mask:0xf\n v_add_f32_dpp %1, %1, %1 row_ror:8 row_mask:0xf bank_mask:0xf\n"
               "v_add_f32_dpp %2, %2, %2 row_ror:8 row_mask:0xf bank_mask:0xf\n v_add_f32_dpp %3, %3, %3 row_ror:8 row_mask:0xf bank_mask:0xf\n"
               "v_add_f32_dpp %8, %8, %8 row_half_mirror row_mask:0xf bank_mask:0xf\n"
               "v_add_f32_dpp %4, %4, %4 row_ror:8 row_mask:0xf bank_mask:0xf\n v_add_f32_dpp %5, %5, %5 row_ror:8 row_mask:0xf bank_mask:0xf\n"
               "v_add_f32_dpp %6, %6, %6 row_ror:8 row_mask:0xf bank_mask:0xf\n v_add_f32_dpp %7, %7, %7 row_ror:8 row_mask:0xf bank_mask:0xf\n"
               : "+v"(acc[16]), "+v"(acc[17]), "+v"(acc[18]), "+v"(acc[19]), "+v"(acc[20]), "+v"(acc[21]), "+v"(acc[22]), "+v"(acc[23]), "+v"(l));
  };
  auto red_b = [&]() {
    float t4[4], u4[4];
    l = red32(l, l);
#pragma unroll
    for (int e = 0; e < 4; ++e) { t4[e] = red32(t8[e], t8[e + 4]); u4[e] = red32(acc[16 + e], acc[20 + e]); }
    l = red16(l, l);
#pragma unroll
    for (int e = 0; e < 4; ++e) o4[e] = red16(t4[e], u4[e]);
  };

  u32 cty = nty, openB = FA;
  flag[0] = ld_g(rws, vo_flag); flag[1] = ld_g(rws, vo_flag + 1024);
  __builtin_amdgcn_sched_barrier(0);
  {
    const rsrc_t rn = next_rsrc();
    kunit(rn, qa, wave, true);
  }
  if (wave >= 4) publish(256u);
  static_assert(AR >= 3 && AR <= 5, "");
  kscore(pf, qa, 8u + wave);
  kscore(pf + 6, qa, 16u + wave);
  if (AR > 3) kscore(tw, qa, 24u + wave);
  if (AR > 4) kscore(tw + 6, qa, 32u + wave);
  qwait(1024u);
#pragma unroll
  for (int j = 0; j < 3; ++j) qf[j] = *reinterpret_cast<const u32x4*>(&s_q[1][j * 64 + c * 8]);
  if (AR <= 3) kscore(tw, qf, 24u + wave);
  if (AR <= 4) kscore(tw + 6, qf, 32u + wave);
  if (XU > 0) kscore(xu, qf, 40u + wave);
  if (XU > 1) kscore(xu + 6, qf, 48u + wave);
  resolve();
  fok = flags_ok() ? 1u : 0u;
  __builtin_amdgcn_sched_barrier(0);
  for (;;) {
    while ((cty & (FK | openB)) == FK) {
      const rsrc_t rn = next_rsrc();
      const bool more = (noff < OOB) & ((nty & FP) == 0u);
      kunit(rn, qf, cty & 255u, more);
      cty = noff < OOB ? nty : FA | 255u;
      resolve();
      __builtin_amdgcn_sched_barrier(0);
    }
    while (((cty & (FK | openB)) == 0u) & (cty != (FA | 255u))) {
      const rsrc_t rn = next_rsrc();
      const bool more = (noff < OOB) & ((nty & FP) == 0u);
      const u32 cslot = cty & 255u;
      POLL_FENCE();
      u32 sb = __hip_atomic_load(reinterpret_cast<u32*>(smb + a_sc + 64u * cslot), __ATOMIC_RELAXED, __HIP_MEMORY_SCOPE_WORKGROUP);
      if (__builtin_expect(__any(sb == SENT), 0)) {
#pragma unroll 1
        for (int spin = 0; spin < SPIN_MAX * 4; ++spin) {
          POLL_FENCE();
          sb = __hip_atomic_load(reinterpret_cast<u32*>(smb + a_sc + 64u * cslot), __ATOMIC_RELAXED, __HIP_MEMORY_SCOPE_WORKGROUP);
          if (!__any(sb == SENT)) break;
          __builtin_amdgcn_s_sleep(1);
        }
      }
      POLL_FENCE();
      draw(more);
      const float sc = bitsf(sb);
      float bm = fmaxf(sc, dpp<HMIR>(sc));
      bm = fmaxf(bm, dpp<ROR8>(bm));
      {
        auto s1 = __builtin_amdgcn_permlane16_swap(__float_as_uint(bm), __float_as_uint(bm), false, false);
        bm = fmaxf(__uint_as_float(s1[0]), __uint_as_float(s1[1]));
        auto s2 = __builtin_amdgcn_permlane32_swap(__float_as_uint(bm), __float_as_uint(bm), false, false);
        bm = fmaxf(__uint_as_float(s2[0]), __uint_as_float(s2[1]));
      }
      const float mn = fmaxf(m, bm);
      const float alpha = __builtin_amdgcn_exp2f((m - mn) * LOG2E);
      const float p = __builtin_amdgcn_exp2f((sc - mn) * LOG2E);
      l = fmaf(l, alpha, p);
      m = mn;
#pragma unroll
      for (int n = 0; n < 24; ++n) acc[n] *= alpha;
      float pb0 = dpp_upd<HMIR, 0xA>(p, p), pb1 = dpp_upd<HMIR, 0x5>(p, p);
      PIN(pb0); PIN(pb1);
      __builtin_amdgcn_sched_barrier(0);
      vfma<0>(ring, rn, vo, acc + 0, pb0);
      ask(more);
      __builtin_amdgcn_sched_barrier(0);
      vfma<1>(ring, rn, vo, acc + 8, pb0);
      vfma<2>(ring, rn, vo, acc + 16, pb0);
      vfma<3>(ring, rn, vo, acc + 0, pb1);
      vfma<4>(ring, rn, vo, acc + 8, pb1);
      vfma<5>(ring, rn, vo, acc + 16, pb1);
      cty = FA | 255u;
      if (noff >= OOB) break;
      cty = nty;
      resolve();
      __builtin_amdgcn_sched_barrier(0);
    }
    if (__builtin_expect((cty & openB) != 0u, 0)) {
      openB = 0u;
      asm volatile("s_branch pg_x_go\n.p2align 12\npg_x_go:\n" ::: "memory");
      flags_wait();
      red_a();
      red_b();
      *reinterpret_cast<f32x4*>(smb + a_part + 8192u + 1024u * wave) = o4;
      if (lane == 0) sm.s_mlb[wave] = f32x2{m, l};
      POLL_FENCE();
      const u32 eb = __hip_atomic_fetch_add(lane == 0 ? &sm.s_exb : &s_cnt[lane], 1u, __ATOMIC_RELAXED, __HIP_MEMORY_SCOPE_WORKGROUP);
      POLL_FENCE();
      if (__builtin_amdgcn_readfirstlane(eb) == 7u) {
        const f32x2 mj = sm.s_mlb[lane & 7];
        f32x4 pt[8];
#pragma unroll
        for (int j = 0; j < 8; ++j) pt[j] = *reinterpret_cast<const f32x4*>(smb + a_part + 8192u + 1024u * j);
        float M = max_dpp<XOR1>(mj[0]); M = max_dpp<XOR2>(M); M = max_dpp<HMIR>(M);
        const float ej = __builtin_amdgcn_exp2f((mj[0] - M) * LOG2E);
        f32x4 o = pt[0] * dpp<0x150>(ej);
        o += pt[1] * dpp<0x151>(ej); o += pt[2] * dpp<0x152>(ej); o += pt[3] * dpp<0x153>(ej);
        o += pt[4] * dpp<0x154>(ej); o += pt[5] * dpp<0x155>(ej); o += pt[6] * dpp<0x156>(ej); o += pt[7] * dpp<0x157>(ej);
        float L = ej * mj[1];
        L += dpp<XOR1>(L); L += dpp<XOR2>(L); L += dpp<HMIR>(L);
        if (lane == 24) { o[0] = M; o[1] = L; }
        st_g(rws, h1o < OOB ? h1o - my_h1 + my_h1b : OOB, u32x4{fbits(o[0]) | 1u, fbits(o[1]) | 1u, fbits(o[2]) | 1u, fbits(o[3]) | 1u});
      }
      m = -INFINITY; l = 0.f;
#pragma unroll
      for (int n = 0; n < 24; ++n) acc[n] = 0.f;
#pragma unroll
      for (int j = 0; j < 3; ++j) qf[j] = *reinterpret_cast<const u32x4*>(&s_q[0][j * 64 + c * 8]);
    }
    if (cty == (FA | 255u)) break;
  }
  STAMP(4);
  __builtin_amdgcn_sched_barrier(0);
  const u32 exv = __hip_atomic_fetch_add(reinterpret_cast<u32*>(smb + a_ex), one, __ATOMIC_RELAXED, __HIP_MEMORY_SCOPE_WORKGROUP);
  POLL_FENCE();
  u32 dnv = __hip_atomic_load(&sm.s_dn, __ATOMIC_RELAXED, __HIP_MEMORY_SCOPE_WORKGROUP);
  POLL_FENCE();
  f32x2 mlj = *reinterpret_cast<const f32x2*>(smb + a_ml);
  f32x4 part[7];
#pragma unroll
  for (int j = 0; j < 7; ++j) part[j] = *reinterpret_cast<const f32x4*>(smb + a_part + 1024 * j);
  POLL_FENCE();
  __builtin_amdgcn_sched_barrier(0);
  red_a();
  __builtin_amdgcn_sched_barrier(0);
  const u32 ex = __builtin_amdgcn_readfirstlane(exv);
  u32 rt = 0u;
  if (__builtin_expect(ex == 0u, 0)) {
    rt = __builtin_amdgcn_raw_ptr_buffer_atomic_add_i32(1, rws, lane == 0 ? OFF_CNT + GRP(bid) * 1024 : OOB, 0, AUX_SC1);
  }
  __builtin_amdgcn_sched_barrier(0);
  const bool own = (lane & 7) == 7;
  u32x4 h1v;
  auto total = [&]() {
    const float mj = own ? m : mlj[0];
    float M = max_dpp<XOR1>(mj); M = max_dpp<XOR2>(M); M = max_dpp<HMIR>(M);
    const float ej = __builtin_amdgcn_exp2f((mj - M) * LOG2E);
    f32x4 o = part[0] * dpp<0x150>(ej);
    o += part[1] * dpp<0x151>(ej); o += part[2] * dpp<0x152>(ej); o += part[3] * dpp<0x153>(ej);
    o += part[4] * dpp<0x154>(ej); o += part[5] * dpp<0x155>(ej); o += part[6] * dpp<0x156>(ej);
    float L = ej * (own ? l : mlj[1]);
    PIN(L);
    L += dpp<XOR1>(L); L += dpp<XOR2>(L); L += dpp<HMIR>(L);
    o += o4 * dpp<0x157>(ej);
    if (lane == 24) { o[0] = M; o[1] = L; }
    h1v = u32x4{fbits(o[0]) | 1u, fbits(o[1]) | 1u, fbits(o[2]) | 1u, fbits(o[3]) | 1u};
  };
  red_b();
  total();
  PIN(h1v);
  if (__builtin_expect(ex == 7u, 1)) {
    if (__builtin_expect(__builtin_amdgcn_readfirstlane(dnv) == 7u, 1)) {
      st_g(rws, h1o, h1v);
    } else {
#pragma unroll 1
      for (int spin = 0; spin < SPIN_MAX * 4; ++spin) {
        POLL_FENCE();
        dnv = __hip_atomic_load(&sm.s_dn, __ATOMIC_RELAXED, __HIP_MEMORY_SCOPE_WORKGROUP);
        if (__builtin_amdgcn_readfirstlane(dnv) == 7u) break;
      }
      POLL_FENCE();
      mlj = *reinterpret_cast<const f32x2*>(smb + a_ml);
#pragma unroll
      for (int j = 0; j < 7; ++j) part[j] = *reinterpret_cast<const f32x4*>(smb + a_part + 1024 * j);
      POLL_FENCE();
      total();
      st_g(rws, h1o, h1v);
    }
  } else {
    *reinterpret_cast<f32x4*>(smb + a_part + 1024 * ex) = o4;
    if (lane == 0) sm.s_ml[ex] = f32x2{m, l};
    POLL_FENCE();
    __hip_atomic_fetch_add(lane == 0 ? &sm.s_dn : &s_cnt[lane], 1u, __ATOMIC_RELAXED, __HIP_MEMORY_SCOPE_WORKGROUP);
    POLL_FENCE();
    if (ex == 0u) { PIN(rt); if (lane == 0) __hip_atomic_store(&sm.s_role, rt + 1u, __ATOMIC_RELAXED, __HIP_MEMORY_SCOPE_WORKGROUP); }
  }
  rt = 0u;
#pragma unroll 1
  for (int spin = 0; spin < SPIN_MAX * 4; ++spin) {
    POLL_FENCE();
    rt = __builtin_amdgcn_readfirstlane(__hip_atomic_load(&sm.s_role, __ATOMIC_RELAXED, __HIP_MEMORY_SCOPE_WORKGROUP));
    if (rt != 0u) break;
    __builtin_amdgcn_s_sleep(1);
  }
  POLL_FENCE();
  rt -= 1u;
#define GETST() do { } while (0)
  {
    const u32 gx = bid & 7;
    rt = rt - G_M < 6u ? T_M + 6 * gx + (rt - G_M) : (rt - G_O < 20u ? T_O + 20 * gx + (rt - G_O) : (rt - G_F < 5u ? T_F + 5 * gx + (rt - G_F) : 255u));
  }
  if (rt >= 248u) { RING_USE(); return; }
  const int mm = lane & 15, kq = lane >> 4;
  const f32x4 zero4 = {0.f, 0.f, 0.f, 0.f};
  const u32 h2x = OFF_H2X + (u32)(bid & (NCP - 1)) * H2B;

  if (rt - T_O < 160u) {
    PAGE("pg_o");
    const int g = (rt - T_O) / 10u, ct = (rt - T_O) % 10u;
    u32x2 wb[3][4];
    if (ex < 4u) {
      u32x2 wl[2][3][4];
      {
        const rsrc_t ra = mk_rsrc(wo + (size_t)(384 * g + 48 * ex) * DM, 240 * DM * 2);
        const u32 va = (u32)((12 * kq * DM + 64 * ct + 4 * mm) * 2);
#pragma unroll
        for (int hd = 1; hd >= 0; --hd)
#pragma unroll
          for (int c3 = 0; c3 < 3; ++c3)
#pragma unroll
            for (int j = 0; j < 4; ++j) wl[hd][c3][j] = __builtin_amdgcn_raw_buffer_load_b64(ra, va, (192 * hd + 4 * c3 + j) * DM * 2, AUX_NT);
      }
      __builtin_amdgcn_sched_barrier(0);
#pragma unroll
      for (int hd = 1; hd >= 0; --hd) {
#pragma unroll
        for (int c3 = 0; c3 < 3; ++c3)
#pragma unroll
          for (int p = 0; p < 2; ++p) {
            wb[c3][0][p] = pack_lo(wl[hd][c3][2 * p][0], wl[hd][c3][2 * p + 1][0]); wb[c3][1][p] = pack_hi(wl[hd][c3][2 * p][0], wl[hd][c3][2 * p + 1][0]);
            wb[c3][2][p] = pack_lo(wl[hd][c3][2 * p][1], wl[hd][c3][2 * p + 1][1]); wb[c3][3][p] = pack_hi(wl[hd][c3][2 * p][1], wl[hd][c3][2 * p + 1][1]);
          }
        if (hd == 1) {
#pragma unroll
          for (int c3 = 0; c3 < 3; ++c3)
#pragma unroll
            for (int t = 0; t < 2; ++t) sm.s_wb[ex][2 * c3 + t][lane] = u32x4{wb[c3][2 * t][0], wb[c3][2 * t][1], wb[c3][2 * t + 1][0], wb[c3][2 * t + 1][1]};
        }
      }
#pragma unroll
      for (int c3 = 0; c3 < 3; ++c3)
#pragma unroll
        for (int t = 0; t < 4; ++t) PIN(wb[c3][t]);
      STAMP(12);
    }
    const int sh = mm >> 3, bb = (mm >> 2) & 1, c3l = mm & 3;
    const u32 sl = (u32)(32 * bb + 2 * g) + (ex >> 2);
    u32 ob = OFF_H1 + (12 * sl + sh) * 1024 + (c3l < 3 ? (u32)(12 * (ex & 3u) + c3l + 3 * kq) * 16 : 768u);
    u32 ob1 = ob + 4096, ob2 = ob + 8192;
    const u32x4 z4 = {0u, 0u, 0u, 0u}, none = {c3l < 3 ? 1u : 0xff800000u, 1u, 1u, 1u};
    u32x4 pa[6];
    pa[0] = z4; pa[1] = z4; pa[2] = z4; pa[3] = sl >= 42u ? z4 : none; pa[4] = sl > 42u ? z4 : none; pa[5] = pa[4];
    u64 m0 = ~0ull, m1 = ~0ull, m2 = ~0ull, m3 = __builtin_amdgcn_ballot_w64(sl >= 42u), m4 = __builtin_amdgcn_ballot_w64(sl > 42u), m5 = m4;
    PIN(ob); PIN(ob1); PIN(ob2); PIN(pa[3]); PIN(pa[4]); PIN(pa[5]);
    asm volatile("s_waitcnt lgkmcnt(0)\n s_barrier" ::: "memory");
    if (ex >= 4u) {
#pragma unroll
      for (int c3 = 0; c3 < 3; ++c3)
#pragma unroll
        for (int t = 0; t < 2; ++t) {
          const u32x4 v = sm.s_wb[ex - 4u][2 * c3 + t][lane];
          wb[c3][2 * t] = u32x2{v[0], v[1]}; wb[c3][2 * t + 1] = u32x2{v[2], v[3]};
        }
    }
#pragma unroll 1
    for (int spin = 0; spin < SPIN_MAX; ++spin) {
      asm volatile("s_mov_b64 exec, %11\n buffer_load_dwordx4 %5, %14, %15, 0 offen offset:2048 sc1\n"
                   "s_mov_b64 exec, %10\n buffer_load_dwordx4 %4, %14, %15, 0 offen sc1\n"
                   "s_mov_b64 exec, %9\n buffer_load_dwordx4 %3, %13, %15, 0 offen offset:2048 sc1\n"
                   "s_mov_b64 exec, %8\n buffer_load_dwordx4 %2, %13, %15, 0 offen sc1\n"
                   "s_mov_b64 exec, %7\n buffer_load_dwordx4 %1, %12, %15, 0 offen offset:2048 sc1\n"
                   "s_mov_b64 exec, %6\n buffer_load_dwordx4 %0, %12, %15, 0 offen sc1\n"
                   "s_mov_b64 exec, -1\n s_waitcnt vmcnt(0)\n"
                   : "+v"(pa[0]), "+v"(pa[1]), "+v"(pa[2]), "+v"(pa[3]), "+v"(pa[4]), "+v"(pa[5])
                   : "s"(m0), "s"(m1), "s"(m2), "s"(m3), "s"(m4), "s"(m5), "v"(ob), "v"(ob1), "v"(ob2), "s"(rsw) : "memory");
      m0 = __builtin_amdgcn_ballot_w64(min4(pa[0]) == 0u); m1 = __builtin_amdgcn_ballot_w64(min4(pa[1]) == 0u);
      m2 = __builtin_amdgcn_ballot_w64(min4(pa[2]) == 0u); m3 = __builtin_amdgcn_ballot_w64(min4(pa[3]) == 0u);
      m4 = __builtin_amdgcn_ballot_w64(min4(pa[4]) == 0u); m5 = __builtin_amdgcn_ballot_w64(min4(pa[5]) == 0u);
      if ((m0 | m1 | m2 | m3 | m4 | m5) == 0ull) break;
      __builtin_amdgcn_s_sleep(PSLEEP);
    }
    RING_USE();
    LSTAMP(6);
    {
      float M = fmaxf(fmaxf(fmaxf(bitsf(pa[0][0]), bitsf(pa[1][0])), fmaxf(bitsf(pa[2][0]), bitsf(pa[3][0]))), fmaxf(bitsf(pa[4][0]), bitsf(pa[5][0])));
      M = fmaxf(M, dpp<ROR8>(M));
      float e[6];
#pragma unroll
      for (int j = 0; j < 6; ++j) e[j] = __builtin_amdgcn_exp2f((bitsf(pa[j][0]) - M) * LOG2E);
      float L = (fmaf(e[0], bitsf(pa[0][1]), e[1] * bitsf(pa[1][1])) + fmaf(e[2], bitsf(pa[2][1]), e[3] * bitsf(pa[3][1]))) + fmaf(e[4], bitsf(pa[4][1]), e[5] * bitsf(pa[5][1]));
      L += dpp<ROR8>(L);
      float w[6], a[4];
#pragma unroll
      for (int j = 0; j < 6; ++j) w[j] = dpp<0xFF>(e[j]);
      const float ri = dpp<0xFF>(__builtin_amdgcn_rcpf(L));
#pragma unroll
      for (int k = 0; k < 4; ++k) {
        a[k] = (fmaf(w[0], bitsf(pa[0][k]), w[1] * bitsf(pa[1][k])) + fmaf(w[2], bitsf(pa[2][k]), w[3] * bitsf(pa[3][k]))) + fmaf(w[4], bitsf(pa[4][k]), w[5] * bitsf(pa[5][k]));
        a[k] += dpp<ROR8>(a[k]);
        a[k] *= ri;
      }
      const u32x2 av = {f2bf(a[0]) | (f2bf(a[1]) << 16), f2bf(a[2]) | (f2bf(a[3]) << 16)};
      f32x4 y;
#pragma unroll
      for (int t = 0; t < 4; ++t) {
        const f32x4 d0 = mfma16(av, wb[0][t], zero4), d1 = mfma16(av, wb[1][t], zero4), d2 = mfma16(av, wb[2][t], zero4);
        y[t] = (d0[0] + d1[1]) + d2[2];
      }
      if (lane < 32) *reinterpret_cast<f32x4*>(&s_y[wave][kq * 64 + 4 * mm]) = y;
    }
    __syncthreads();
    if (wave < NCP) {
      f32x2 p[WAVES];
#pragma unroll
      for (int w = 0; w < WAVES; ++w) p[w] = *reinterpret_cast<const f32x2*>(&s_y[w][2 * lane]);
      const float y0 = ((p[0][0] + p[1][0]) + (p[2][0] + p[3][0])) + ((p[4][0] + p[5][0]) + (p[6][0] + p[7][0]));
      const float y1 = ((p[0][1] + p[1][1]) + (p[2][1] + p[3][1])) + ((p[4][1] + p[5][1]) + (p[6][1] + p[7][1]));
      __builtin_amdgcn_raw_buffer_store_b64(u32x2{fbits(y0) | 1u, fbits(y1) | 1u}, rws, OFF_H2X + (u32)wave * H2B + (u32)((g * 2 + (lane >> 5)) * DM + 64 * ct + 2 * (lane & 31)) * 4, 0, AUX_SC1);
    }
    LSTAMP(7);
    DUMP();
    return;
  }

  if (rt - T_M < 48u) {
    PAGE("pg_m");
    RING_USE();
    const int mi = (int)(rt - T_M);
    u32 l1[5][4];
    u32x4 l2[8];
    u32 l3[8];
    {
      const rsrc_t ra = mk_rsrc(w1 + (size_t)(80 * wave) * FF, 80 * FF * 2);
      const u32 va = (u32)((20 * kq * FF + 32 * mi + 2 * mm) * 2);
#pragma unroll
      for (int c5 = 0; c5 < 5; ++c5)
#pragma unroll
        for (int j = 0; j < 4; ++j) l1[c5][j] = __builtin_amdgcn_raw_buffer_load_b32(ra, va, (4 * c5 + j) * FF * 2, W1AUX);
      const rsrc_t rb = mk_rsrc(w2 + (size_t)(32 * mi) * DM, 32 * DM * 2);
      const u32 vb_ = wave < 4 ? (u32)((8 * kq * DM + 160 * wave + 8 * mm) * 2) : OOB;
      const u32 vc_ = wave < 4 ? (u32)((8 * kq * DM + 160 * wave + 128 + 2 * mm) * 2) : OOB;
#pragma unroll
      for (int k = 0; k < 8; ++k) l2[k] = __builtin_amdgcn_raw_buffer_load_b128(rb, vb_, k * DM * 2, AUX_NT);
#pragma unroll
      for (int k = 0; k < 8; ++k) l3[k] = __builtin_amdgcn_raw_buffer_load_b32(rb, vc_, k * DM * 2, AUX_NT);
    }
    const int bb = mm >> 3, c5l = mm & 7;
    const bool act = c5l < 5;
    const u32 xo = act ? (u32)(bb * DM + 80 * wave + 4 * c5l + 20 * kq) * 2 : OOB;
    const u32x2 xres = __builtin_amdgcn_raw_buffer_load_b64(mk_rsrc(x, 2 * DM * 2), xo, 0, 0);
    const u32 bias_raw = __builtin_amdgcn_raw_buffer_load_b16(mk_rsrc(b1, FF * 2), (u32)(32 * mi + (lane & 31)) * 2, 0, 0);
    __builtin_amdgcn_sched_barrier(0);
    u32x2 w1b[5][2];
    u32x4 w2b[10];
#pragma unroll
    for (int c5 = 0; c5 < 5; ++c5) {
      w1b[c5][0] = u32x2{pack_lo(l1[c5][0], l1[c5][1]), pack_lo(l1[c5][2], l1[c5][3])};
      w1b[c5][1] = u32x2{pack_hi(l1[c5][0], l1[c5][1]), pack_hi(l1[c5][2], l1[c5][3])};
      PIN(w1b[c5][0]); PIN(w1b[c5][1]);
    }
#pragma unroll
    for (int k = 0; k < 4; ++k)
#pragma unroll
      for (int p = 0; p < 4; ++p) {
        w2b[2 * k][p] = pack_lo(l2[2 * p][k], l2[2 * p + 1][k]); w2b[2 * k + 1][p] = pack_hi(l2[2 * p][k], l2[2 * p + 1][k]);
      }
#pragma unroll
    for (int p = 0; p < 4; ++p) { w2b[8][p] = pack_lo(l3[2 * p], l3[2 * p + 1]); w2b[9][p] = pack_hi(l3[2 * p], l3[2 * p + 1]); }
#pragma unroll
    for (int t = 0; t < 10; ++t) PIN(w2b[t]);
    float colsum = 0.f;
    {
      const u32x2 ones = {0x3f803f80u, 0x3f803f80u};
      f32x4 s0 = zero4, s1 = zero4;
#pragma unroll
      for (int c5 = 0; c5 < 5; ++c5) { s0 = mfma16(ones, w1b[c5][0], s0); s1 = mfma16(ones, w1b[c5][1], s1); }
      if (lane < 32) s_S[wave][2 * mm + kq] = kq ? s1[0] : s0[0];
      __syncthreads();
      GETST();
#pragma unroll
      for (int w8 = 0; w8 < WAVES; ++w8) colsum += s_S[w8][lane & 31];
    }
    float bias = bf2f(bias_raw);
    float xr[4] = {bflo(xres[0]), bfhi(xres[0]), bflo(xres[1]), bfhi(xres[1])};
    u32 amask = act ? ~0u : 0u;
    PIN(colsum); PIN(bias); PIN(xr[0]); PIN(xr[1]); PIN(xr[2]); PIN(xr[3]); PIN(amask);
    STAMP(13);
    float hv[4];
    {
      u32x4 pa[16];
      poll<16>(pa, rws, act ? h2x + xo * 2 : OOB, 2 * DM * 4);
#pragma unroll
      for (int k = 0; k < 4; ++k) {
        float p[8];
#pragma unroll
        for (int q = 0; q < 8; ++q) p[q] = bitsf(pa[2 * q][k]) + bitsf(pa[2 * q + 1][k]);
        hv[k] = rbf(xr[k] + rbf(((p[0] + p[1]) + (p[2] + p[3])) + ((p[4] + p[5]) + (p[6] + p[7]))));
      }
    }
    LSTAMP(8);
    {
      const u32x2 av = {(fbits(hv[0]) >> 16) | (fbits(hv[1]) & 0xffff0000u), (fbits(hv[2]) >> 16) | (fbits(hv[3]) & 0xffff0000u)};
      float z[2];
#pragma unroll
      for (int t = 0; t < 2; ++t) {
        const f32x4 d0 = mfma16(av, w1b[0][t], zero4), d1 = mfma16(av, w1b[1][t], zero4), d2 = mfma16(av, w1b[2][t], zero4);
        const f32x4 d3 = mfma16(av, w1b[3][t], zero4), d4 = mfma16(av, w1b[4][t], zero4);
        z[t] = (kq & 1) ? d4[0] : (d0[0] + d1[1]) + (d2[2] + d3[3]);
      }
      const u32 r0 = __builtin_amdgcn_readlane(fbits(hv[0]), 0), r1 = __builtin_amdgcn_readlane(fbits(hv[0]), 8);
      const float ref = bitsf((bb ? r1 : r0) & amask);
      const float e0 = hv[0] - ref, e1 = hv[1] - ref, e2 = hv[2] - ref, e3 = hv[3] - ref;
      float t1 = (e0 + e1) + (e2 + e3), t2 = fmaf(e0, e0, e1 * e1) + fmaf(e2, e2, e3 * e3);
      t1 = red32(t1, t1); t2 = red32(t2, t2);
      t1 = red16(t1, t1); t2 = red16(t2, t2);
      t1 += dpp<XOR1>(t1); t2 += dpp<XOR1>(t2);
      t1 += dpp<XOR2>(t1); t2 += dpp<XOR2>(t2);
      t1 += dpp<HMIR>(t1); t2 += dpp<HMIR>(t2);
      const float dm_ = t1 * (1.0f / 80.f);
      if ((lane & 55) == 0) s_st[2 * wave + bb] = f32x2{ref + dm_, fmaf(-t1, dm_, t2)};
      s_y[wave][(kq >> 1) * 32 + 2 * mm + (kq & 1)] = red16(z[0], z[1]);
    }
    __syncthreads();
    LSTAMP(14);
    if (wave < 4) {
      {
        const f32x2 st = s_st[2 * (lane & 7) + (lane >> 5)];
        float sacc = 0.f;
#pragma unroll
        for (int w = 0; w < WAVES; ++w) sacc += s_y[w][lane];
        float mean = st[0];
        mean += dpp<XOR1>(mean); mean += dpp<XOR2>(mean); mean += dpp<HMIR>(mean);
        mean *= 0.125f;
        const float dd = st[0] - mean;
        float M2 = fmaf(80.f * dd, dd, st[1]);
        M2 += dpp<XOR1>(M2); M2 += dpp<XOR2>(M2); M2 += dpp<HMIR>(M2);
        const float den = rbf(__builtin_amdgcn_sqrtf(rbf(rbf(M2 * (1.0f / DM)) + 1e-5f)));
        const float hh = rbf(rbf((sacc - rbf(mean) * colsum) * __builtin_amdgcn_rcpf(den)) + bias);
        const float rr = hh > 0.f ? hh : 0.f;
        s_a[wave][lane >> 5][lane & 31] = (u16)f2bf(rr * rr);
      }
      __builtin_amdgcn_wave_barrier();
      const u32x4 av = *reinterpret_cast<const u32x4*>(&s_a[wave][mm & 1][8 * kq]);
      f32x4 d[10];
#pragma unroll
      for (int t = 0; t < 10; ++t) d[t] = mfma32(av, w2b[t], zero4);
      const bool k1 = kq & 1, k2 = kq & 2;
      float v[4];
#pragma unroll
      for (int e = 0; e < 4; ++e) {
        const int t = e & 1, i = e >> 1;
        const float lo = k1 ? d[2 + t][i] : d[t][i], hi = k1 ? d[6 + t][i] : d[4 + t][i];
        v[e] = k2 ? hi : lo;
      }
      const u32 h3 = OFF_H3 + (u32)mi * (2 * DM * 4) + (u32)(80 * wave + lane) * 16;
      st_g(rws, h3, u32x4{fbits(v[0]) | 1u, fbits(v[1]) | 1u, fbits(v[2]) | 1u, fbits(v[3]) | 1u});
      st_g(rws, lane < 16 ? h3 + 1024 : OOB, u32x4{fbits(d[8][0]) | 1u, fbits(d[9][0]) | 1u, fbits(d[8][1]) | 1u, fbits(d[9][1]) | 1u});
    }
    STAMP(11);
    DUMP();
    return;
  }

  {
    PAGE("pg_f");
    RING_USE();
    const int fq = (int)(rt - T_F), fu = fq % 10;
    const int col = 160 * (fq / 10) + (fu < 8 ? 64 * (fu & 1) + 2 * (fu >> 1) + 8 * c : 128 + 16 * (fu - 8) + 2 * c);
    u32 touch = __builtin_amdgcn_raw_buffer_load_b32(mk_rsrc(out, 2 * DM * 2), wave == 1 ? (u32)((lane >> 5) * DM + col) * 2 : OOB, 0, 0);
    if (wave == 7) {
      const u32 xo = lane < 16 ? (u32)((lane >> 3) * DM + (col & ~3)) * 2 : OOB;
      const u32x2 xres = __builtin_amdgcn_raw_buffer_load_b64(mk_rsrc(x, 2 * DM * 2), xo, 0, 0);
      const u32 bias_raw = __builtin_amdgcn_raw_buffer_load_b32(mk_rsrc(b2, DM * 2), (u32)col * 2, 0, 0);
      u32x4 pa[16];
      poll<16>(pa, rws, lane < 16 ? h2x + xo * 2 : OOB, 2 * DM * 4);
      const bool up = col & 2;
      const u32 xp = up ? xres[1] : xres[0];
      float hv[2];
#pragma unroll
      for (int e = 0; e < 2; ++e) {
        float p[8];
#pragma unroll
        for (int q = 0; q < 8; ++q) p[q] = bitsf(up ? pa[2 * q][2 + e] : pa[2 * q][e]) + bitsf(up ? pa[2 * q + 1][2 + e] : pa[2 * q + 1][e]);
        const float sa = ((p[0] + p[1]) + (p[2] + p[3])) + ((p[4] + p[5]) + (p[6] + p[7]));
        hv[e] = rbf((e ? bfhi(xp) : bflo(xp)) + rbf(sa));
      }
      if (lane < 16) {
        *reinterpret_cast<f32x2*>(&s_hres[2 * lane]) = f32x2{hv[0], hv[1]};
        *reinterpret_cast<f32x2*>(&s_bias[2 * lane]) = f32x2{bflo(bias_raw), bfhi(bias_raw)};
      }
    }
    __syncthreads();
    GETST();
    if (wave == 0) {
      const int e = (lane >> 4) & 1, idx = 16 * (lane >> 5) + 2 * c + e;
      float hres = s_hres[idx], bias = s_bias[idx];
      PIN(hres); PIN(bias);
      u32x4 pw[6];
      poll<6>(pw, rws, OFF_H3 + (u32)r * (2 * DM * 4) + (u32)(8 * fq + c) * 16, 8 * 2 * DM * 4);
      LSTAMP(9);
      float y[4];
#pragma unroll
      for (int k = 0; k < 4; ++k)
        y[k] = ((bitsf(pw[0][k]) + bitsf(pw[1][k])) + (bitsf(pw[2][k]) + bitsf(pw[3][k]))) + (bitsf(pw[4][k]) + bitsf(pw[5][k]));
      const float u0 = red32(y[0], y[2]), u1 = red32(y[1], y[3]);
      float t = red16(u0, u1);
      t += dpp<ROR8>(t);
      const float o = rbf(rbf(t) + bias);
      __builtin_amdgcn_raw_buffer_store_b16((u16)f2bf(hres + o), mk_rsrc(out, 2 * DM * 2), (lane & 8) == 0 ? (u32)((lane >> 5) * DM + col + e) * 2 : OOB, 0, OUT_AUX);
    }
    STAMP(10);
    DUMP();
    PIN(touch);
    if (fq == 0 && tid == 0) __builtin_amdgcn_raw_buffer_store_b64(u32x2{tagA, tagB}, rws, OFF_EPOCH, 0, AUX_SC1);
  }
}

extern "C" void kernel_launch(void* const* d_in, const int* in_sizes, int n_in,
                              void* d_out, int out_size, void* d_ws, size_t ws_size,
                              hipStream_t stream) {
  fused_kernel<<<256, 512, 0, stream>>>((const u16*)d_in[0], (const u16*)d_in[1], (const u16*)d_in[2], (const u16*)d_in[3],
                                        (const u16*)d_in[4], (const u16*)d_in[6], (const u16*)d_in[7], (const u16*)d_in[8],
                                        (const u16*)d_in[9], (unsigned char*)d_ws, (u16*)d_out);
}
```
